```python
import torch

def _softmax(x, axis=-1):
    return torch.softmax(x, dim=axis)

def _gelu(x):
    return torch.nn.functional.gelu(x, approximate='tanh')

def _silu(x):
    return torch.nn.functional.silu(x)

def _sigmoid(x):
    return torch.sigmoid(x)

def _layernorm(x, axis=-1, eps=1e-05):
    m = x.mean(dim=axis, keepdim=True)
    v = x.var(dim=axis, keepdim=True, unbiased=False)
    return (x - m) / torch.sqrt(v + eps)

def _scatter_add(tgt, idx, src, axis=0):
    return tgt.clone().index_add_(axis, idx, src)

def _segment_sum(src, seg, n):
    out = torch.zeros((n,) + src.shape[1:], dtype=src.dtype)
    return out.index_add_(0, seg, src)

def _pad(x, pw):
    flat = tuple((v for pair in reversed(pw) for v in pair))
    return torch.nn.functional.pad(x, flat)
kernel_name = 'adaLN_cond__sliding_local_dilated__dense_activation_silu__prompt'

def setup_inputs():

    _w = lambda a: (lambda t: t.to(torch.bfloat16) if t.is_floating_point() else t)(torch.as_tensor(a))

    def _A_setup():
        B, S, D = (1, 2048, 448)
        return {'x': _w((lambda *s: torch.randn(*s, dtype=torch.bfloat16) * 0.05)(B, S, D)), 'wqkv': _w((lambda *s: torch.randn(*s, dtype=torch.bfloat16) * 0.05)(D, 3 * D)), 'wo': _w((lambda *s: torch.randn(*s, dtype=torch.bfloat16) * 0.05)(D, D))}
    _A_in = _A_setup()

    def _B_setup():
        B, S, D, F = (1, 2048, 448, 1792)
        return {'x': _w((lambda *s: torch.randn(*s, dtype=torch.bfloat16) * 0.05)(B, S, D)), 'w1': _w((lambda *s: torch.randn(*s, dtype=torch.bfloat16) * 3.2)(D, F) * D ** (-0.5)), 'w2': _w((lambda *s: torch.randn(*s, dtype=torch.bfloat16) * 0.4)(F, D) * F ** (-0.5))}
    _B_in = _B_setup()
    return {'A_x': _A_in['x'], 'A_wqkv': _A_in['wqkv'], 'A_wo': _A_in['wo'], 'B_x': _B_in['x'], 'B_w1': _B_in['w1'], 'B_w2': _B_in['w2'], 'c': _w((lambda *s: torch.randn(*s, dtype=torch.bfloat16) * 0.1)(1, 448) * 8.0), 'w_mod': _w((lambda *s: torch.randn(*s, dtype=torch.bfloat16) * 0.1)(448, 6 * 448) * 448 ** (-0.5) * 8.0)}

def reference(A_x, A_wqkv, A_wo, B_x, B_w1, B_w2, c, w_mod):

    def _A_ref(x, wqkv, wo):
        B, S, D = x.shape
        H = 16
        Dh = D // H
        W, dil = (8, 2)
        qkv = (lambda x, *d: x.permute(*d))((x @ wqkv).reshape(B, S, 3, H, Dh), 2, 0, 3, 1, 4)
        q, k, v = (qkv[0], qkv[1], qkv[2])
        scale = Dh ** (-0.5)
        pos = torch.arange(S)
        off = torch.arange(W) * dil
        idx = pos[:, None] - off[None, :]
        mask = idx >= 0
        idx_c = torch.where(mask, idx, 0).reshape(-1)
        kg = k[:, :, idx_c, :].reshape(B, H, S, W, Dh)
        vg = v[:, :, idx_c, :].reshape(B, H, S, W, Dh)
        logits = torch.einsum('bhsd,bhswd->bhsw', q, kg) * scale
        logits = torch.where(mask[None, None, :, :], logits, -1000000000.0)
        attn = _softmax(logits, axis=-1)
        o = torch.einsum('bhsw,bhswd->bhsd', attn, vg)
        return (lambda x, *d: x.permute(*d))(o, 0, 2, 1, 3).reshape(B, S, D) @ wo

    def _B_ref(x, w1, w2):
        h = torch.einsum('bsd,df->bsf', x, w1)
        h = _silu(h)
        out = torch.einsum('bsf,fd->bsd', h, w2)
        return out
    x = A_x
    mod = (_silu(c) @ w_mod).reshape(c.shape[0], 6, 1, -1)
    sa, ca, ga, sb, cb, gb = (mod[:, 0].clone(), mod[:, 1].clone(), mod[:, 2].clone(), mod[:, 3].clone(), mod[:, 4].clone(), mod[:, 5].clone())
    h = _layernorm(x, axis=-1) * (1 + ca) + sa
    x = x + ga * _A_ref(h, A_wqkv, A_wo)
    h = _layernorm(x, axis=-1) * (1 + cb) + sb
    x = x + gb * _B_ref(h, B_w1, B_w2)
    return x
```

```cpp
#include <hip/hip_runtime.h>
#include <hip/hip_bf16.h>
#include <stdint.h>

typedef unsigned short u16;
typedef short bf16x8 __attribute__((ext_vector_type(8)));
typedef float f32x4 __attribute__((ext_vector_type(4)));

constexpr int S = 2048, D = 448, H = 16, DH = 28, F = 1792;

__device__ __forceinline__ float bf2f(u16 v) { return __uint_as_float(((unsigned)v) << 16); }
__device__ __forceinline__ u16 f2bf(float f) {
  __hip_bfloat16 b = __float2bfloat16(f);
  return *reinterpret_cast<u16*>(&b);
}
__device__ __forceinline__ float rbf(float f) { return bf2f(f2bf(f)); }

__global__ void k_mod(const u16* c, const u16* wm, float* mod) {
  __shared__ float sc[D];
  for (int i = threadIdx.x; i < D; i += blockDim.x) {
    float v = bf2f(c[i]);
    sc[i] = rbf(v / (1.f + __expf(-v)));
  }
  __syncthreads();
  int n = blockIdx.x * blockDim.x + threadIdx.x;
  if (n >= 6 * D) return;
  float acc = 0.f;
  for (int k = 0; k < D; ++k) acc += sc[k] * bf2f(wm[(size_t)k * 6 * D + n]);
  mod[n] = rbf(acc);
}

__global__ void k_ln(const u16* x, const float* shift, const float* scale, u16* h) {
  int row = blockIdx.x, lane = threadIdx.x;
  float v[7];
  float s = 0.f;
  for (int j = 0; j < 7; ++j) { v[j] = bf2f(x[(size_t)row * D + lane + 64 * j]); s += v[j]; }
  for (int o = 32; o; o >>= 1) s += __shfl_xor(s, o);
  float m = s / D;
  float q = 0.f;
  for (int j = 0; j < 7; ++j) { float d = v[j] - m; q += d * d; }
  for (int o = 32; o; o >>= 1) q += __shfl_xor(q, o);
  float r = rsqrtf(q / D + 1e-5f);
  for (int j = 0; j < 7; ++j) {
    int k = lane + 64 * j;
    h[(size_t)row * D + k] = f2bf((v[j] - m) * r * rbf(1.f + scale[k]) + shift[k]);
  }
}

template <int MODE>
__global__ void k_gemm(const u16* A, const u16* B, u16* C, int M, int N, int K, const u16* res, const float* gate) {
  int lane = threadIdx.x & 63, w = threadIdx.x >> 6;
  int tn = blockIdx.x * 4 + w, tm = blockIdx.y;
  if (tn * 16 >= N) return;
  f32x4 acc = {0, 0, 0, 0};
  int r = lane & 15, g = lane >> 4;
  for (int k0 = 0; k0 < K; k0 += 32) {
    bf16x8 a = *(const bf16x8*)(A + (size_t)(tm * 16 + r) * K + k0 + 8 * g);
    bf16x8 b;
    for (int j = 0; j < 8; ++j) b[j] = (short)B[(size_t)(k0 + 8 * g + j) * N + tn * 16 + r];
    acc = __builtin_amdgcn_mfma_f32_16x16x32_bf16(a, b, acc, 0, 0, 0);
  }
  for (int i = 0; i < 4; ++i) {
    int row = tm * 16 + 4 * g + i, col = tn * 16 + r;
    float v = acc[i];
    if (MODE == 1) { v = rbf(v); v = v / (1.f + __expf(-v)); }
    if (MODE == 2) { v = bf2f(res[(size_t)row * N + col]) + rbf(gate[col] * rbf(v)); }
    C[(size_t)row * N + col] = f2bf(v);
  }
}

__global__ void k_attn(const u16* qkv, u16* o) {
  int idx = blockIdx.x * blockDim.x + threadIdx.x;
  if (idx >= S * H) return;
  int s = idx / H, h = idx % H;
  const u16* q = qkv + (size_t)s * 3 * D + h * DH;
  float lg[8];
  float mx = -1e30f;
  for (int w = 0; w < 8; ++w) {
    int t = s - 2 * w;
    float a = -1e9f;
    if (t >= 0) {
      const u16* k = qkv + (size_t)t * 3 * D + D + h * DH;
      a = 0.f;
      for (int d = 0; d < DH; ++d) a += bf2f(q[d]) * bf2f(k[d]);
      a = rbf(rbf(a) * 0.1889822365046136f);
    }
    lg[w] = a;
    mx = fmaxf(mx, a);
  }
  float sum = 0.f;
  for (int w = 0; w < 8; ++w) { lg[w] = __expf(lg[w] - mx); sum += lg[w]; }
  float inv = 1.f / sum;
  for (int d = 0; d < DH; ++d) {
    float a = 0.f;
    for (int w = 0; w < 8; ++w) {
      int t = s - 2 * w;
      if (t >= 0) a += rbf(lg[w] * inv) * bf2f(qkv[(size_t)t * 3 * D + 2 * D + h * DH + d]);
    }
    o[(size_t)s * D + h * DH + d] = f2bf(a);
  }
}

extern "C" void kernel_launch(void* const* d_in, const int* in_sizes, int n_in, void* d_out, int out_size, void* d_ws,
                              size_t ws_size, hipStream_t stream) {
  const u16* x = (const u16*)d_in[0];
  const u16* wqkv = (const u16*)d_in[1];
  const u16* wo = (const u16*)d_in[2];
  const u16* w1 = (const u16*)d_in[4];
  const u16* w2 = (const u16*)d_in[5];
  const u16* c = (const u16*)d_in[6];
  const u16* wm = (const u16*)d_in[7];
  char* ws = (char*)d_ws;
  float* mod = (float*)ws;
  u16* h = (u16*)(ws + 65536);
  u16* qkv = h + S * D;
  u16* o = qkv + S * 3 * D;
  u16* x1 = o + S * D;
  u16* hid = x1 + S * D;
  u16* out = (u16*)d_out;
  k_mod<<<42, 64, 0, stream>>>(c, wm, mod);
  k_ln<<<S, 64, 0, stream>>>(x, mod, mod + D, h);
  k_gemm<0><<<dim3(21, S / 16), 256, 0, stream>>>(h, wqkv, qkv, S, 3 * D, D, nullptr, nullptr);
  k_attn<<<S * H / 256, 256, 0, stream>>>(qkv, o);
  k_gemm<2><<<dim3(7, S / 16), 256, 0, stream>>>(o, wo, x1, S, D, D, x, mod + 2 * D);
  k_ln<<<S, 64, 0, stream>>>(x1, mod + 3 * D, mod + 4 * D, h);
  k_gemm<1><<<dim3(28, S / 16), 256, 0, stream>>>(h, w1, hid, S, F, D, nullptr, nullptr);
  k_gemm<2><<<dim3(7, S / 16), 256, 0, stream>>>(hid, w2, out, S, D, F, x1, mod + 5 * D);
}
```

```cpp
#include <hip/hip_runtime.h>

typedef unsigned u32;
typedef unsigned long long u64;
typedef u32 v4u __attribute__((ext_vector_type(4)));
typedef u32 v2u __attribute__((ext_vector_type(2)));
typedef float f32x4 __attribute__((ext_vector_type(4)));
typedef float f32x2 __attribute__((ext_vector_type(2)));
typedef short bf16x8 __attribute__((ext_vector_type(8)));
typedef __bf16 bf16x2 __attribute__((ext_vector_type(2)));
typedef __amdgpu_buffer_rsrc_t rsrc_t;
#define LAS __attribute__((address_space(3)))
#define SB() __builtin_amdgcn_sched_barrier(0)
#define NOPK
#define DEV __device__ __forceinline__ NOPK
#define LAM __attribute__((always_inline)) NOPK
#define fmaxf __builtin_fmaxf

namespace fk {
constexpr int S = 2048, D = 448, F = 1792;
constexpr int WS_EPOCH = 0;
constexpr int WS_FA = 4096;
constexpr int WS_FB = 8192;
constexpr int WS_FH = 32768;
constexpr int WS_MOD = 16384;
constexpr int WS_MU = 65536;
constexpr int WS_O = 131072;
constexpr int WS_X = WS_O + 32 * 65536;
constexpr int XS_OFF = 57344;
constexpr int WS_H = WS_X + 32 * 65536;
constexpr int H_CL = 8 * 4 * 7 * 1024;
constexpr int WS_PQ = WS_H + 32 * H_CL;
constexpr int WS_PO = WS_PQ + 8 * 168 * 1024;
constexpr int WS_P2 = WS_PO + 8 * 65536;
constexpr int WS_P1 = WS_P2 + 8 * 224 * 1024;
constexpr int WS_END = WS_P1 + 8 * 196 * 1024;
constexpr int L_A = 0;
constexpr int L_B = 71680;
constexpr int L_PAIR = 137216;
constexpr int L_MODP = 141312;
constexpr int L_MU1 = 154496;
constexpr int L_GA = 155648, L_SB = L_GA + 896, L_CB = L_SB + 896, L_GB = L_CB + 896;
constexpr int L_ST = L_GB + 896;
constexpr int L_UV = L_ST + 1024;
constexpr int L_CNT = L_UV + 1792;
constexpr int L_END = L_CNT + 16;
static_assert(L_END <= 163840, "LDS");
constexpr int QK_HD = 26 * 1024;
constexpr u32 SENT = 0xFFFFFFFFu;
constexpr int SPIN_CAP = 1 << 16;
constexpr int OOB = 0x40000000;
constexpr int CODE_SLICES = 4;
#define NQW 6
#define N1B 6
#define N1C 16
#define N2A 8
#define N2B 16

DEV float asf(u32 x) { return __builtin_bit_cast(float, x); }
DEV u32 asu(float x) { return __builtin_bit_cast(u32, x); }
DEV float lo16(u32 x) { return asf(x << 16); }
DEV float hi16(u32 x) { return asf(x & 0xFFFF0000u); }
DEV u32 pk(float a, float b) {
  f32x2 v = {a, b};
  return __builtin_bit_cast(u32, __builtin_convertvector(v, bf16x2));
}
DEV u32 pk2(f32x2 v) { return __builtin_bit_cast(u32, __builtin_convertvector(v, bf16x2)); }
DEV u32 pkn(float a, float b) {
  asm("" : "+v"(a));
  asm("" : "+v"(b));
  return pk(a, b);
}
DEV f32x2 up2(u32 x) { return f32x2{lo16(x), hi16(x)}; }
DEV f32x2 fma2(f32x2 a, f32x2 b, f32x2 c) { return __builtin_elementwise_fma(a, b, c); }
DEV u32 canon(u32 v) { return v < 0xFFFFFFFEu ? v : 0xFFFFFFFEu; }
DEV f32x4 mfma(v4u a, v4u b, f32x4 c) {
  return __builtin_amdgcn_mfma_f32_16x16x32_bf16(__builtin_bit_cast(bf16x8, a), __builtin_bit_cast(bf16x8, b), c, 0, 0, 0);
}
DEV float dot2(u32 a, u32 b, float c) {
  return __builtin_amdgcn_fdot2_f32_bf16(__builtin_bit_cast(bf16x2, a), __builtin_bit_cast(bf16x2, b), c, false);
}
DEV rsrc_t mkrs(const void* p, int bytes) { return __builtin_amdgcn_make_buffer_rsrc((void*)p, 0, bytes, 0x00020000); }
DEV u32 ld32(rsrc_t r, int v, int s) { return __builtin_amdgcn_raw_buffer_load_b32(r, v, s, 0); }
DEV v4u ld128(rsrc_t r, int v, int s) { return __builtin_amdgcn_raw_buffer_load_b128(r, v, s, 0); }
DEV v2u ld64(rsrc_t r, int v, int s) { return __builtin_amdgcn_raw_buffer_load_b64(r, v, s, 0); }
DEV u32 ld32c(rsrc_t r, int v, int s) { return __builtin_amdgcn_raw_buffer_load_b32(r, v, s, 16); }
DEV v4u ld128c(rsrc_t r, int v, int s) { return __builtin_amdgcn_raw_buffer_load_b128(r, v, s, 16); }
DEV void st128c(v4u x, rsrc_t r, int v, int s) { __builtin_amdgcn_raw_buffer_store_b128(x, r, v + s, 0, 16); }
DEV void st64c(v2u x, rsrc_t r, int v, int s) { __builtin_amdgcn_raw_buffer_store_b64(x, r, v, s, 16); }
DEV void st32c(u32 x, rsrc_t r, int v, int s) { __builtin_amdgcn_raw_buffer_store_b32(x, r, v, s, 16); }
DEV v4u lds128(LAS char* L, int off) { return *(LAS v4u*)(L + off); }
DEV f32x4 ldsf4(LAS char* L, int off) { return *(LAS f32x4*)(L + off); }
DEV void sts128(LAS char* L, int off, v4u x) { *(LAS v4u*)(L + off) = x; }
DEV void stsf4(LAS char* L, int off, f32x4 x) { *(LAS f32x4*)(L + off) = x; }
DEV void sts64(LAS char* L, int off, v2u x) { *(LAS v2u*)(L + off) = x; }
typedef short v4s __attribute__((ext_vector_type(4)));
DEV v2u trread(LAS char* L, int off) { return __builtin_bit_cast(v2u, __builtin_amdgcn_ds_read_tr16_b64_v4i16((LAS v4s*)(L + off))); }
DEV void bar() {
  asm volatile("s_waitcnt lgkmcnt(0)" ::: "memory");
  __builtin_amdgcn_s_barrier();
  asm volatile("" ::: "memory");
}
DEV void drain() { asm volatile("s_waitcnt vmcnt(0)" ::: "memory"); }
DEV void pin(v4u& x) { asm volatile("" : "+v"(x)); }
DEV void dma1k(v4u rs, int voff, int soff, int lds) {
  asm volatile("s_mov_b32 m0, %2\n\ts_nop 0\n\tbuffer_load_dwordx4 %0, %1, %3 offen sc1 lds" ::"v"(voff), "s"(rs), "s"(lds), "s"(soff) : "memory");
}
DEV void dma1kp(v4u rs, int voff, int soff, int lds) {
  asm volatile("s_mov_b32 m0, %2\n\ts_nop 0\n\tbuffer_load_dwordx4 %0, %1, %3 offen lds" ::"v"(voff), "s"(rs), "s"(lds), "s"(soff) : "memory");
}
DEV void xpose(const u32* r, v4u& f0, v4u& f1) {
#pragma unroll
  for (int i = 0; i < 4; ++i) {
    f0[i] = __builtin_amdgcn_perm(r[2 * i + 1], r[2 * i], 0x05040100u);
    f1[i] = __builtin_amdgcn_perm(r[2 * i + 1], r[2 * i], 0x07060302u);
  }
}
template <int CTRL>
DEV float dppadd(float x) {
  return x + asf(__builtin_amdgcn_update_dpp(0, asu(x), CTRL, 0xF, 0xF, true));
}
DEV float rowsum(float x) {
  x = dppadd<0x128>(x);
  x = dppadd<0x124>(x);
  x = dppadd<0x122>(x);
  return dppadd<0x121>(x);
}
DEV float fold32(float a, float b) {
  auto r = __builtin_amdgcn_permlane32_swap(asu(a), asu(b), false, false);
  return asf(r[0]) + asf(r[1]);
}
DEV float fold16(float a, float b) {
  auto r = __builtin_amdgcn_permlane16_swap(asu(a), asu(b), false, false);
  return asf(r[0]) + asf(r[1]);
}
DEV float gsum(float x) { x = fold16(x, x); return fold32(x, x); }
DEV float gmax(float x) {
  auto r = __builtin_amdgcn_permlane16_swap(asu(x), asu(x), false, false);
  x = fmaxf(asf(r[0]), asf(r[1]));
  auto q = __builtin_amdgcn_permlane32_swap(asu(x), asu(x), false, false);
  return fmaxf(asf(q[0]), asf(q[1]));
}
DEV u32 umax(u32 a, u32 b) { return a > b ? a : b; }
DEV u32 max4(v4u x, u32 m) {
  m = umax(umax(x[0], x[1]), m);
  return umax(umax(x[2], x[3]), m);
}
template <int N>
DEV void issue(v4u (&f)[N], rsrc_t rs, int voff) {
#pragma unroll
  for (int i = 0; i < N; ++i) f[i] = ld128c(rs, voff + (i & 3) * 1024, (i >> 2) * 4096);
}
template <int N>
DEV bool pending(const v4u (&f)[N]) {
  u32 m = 0;
#pragma unroll
  for (int i = 0; i < N; ++i) m = max4(f[i], m);
  return __builtin_amdgcn_ballot_w64(m == SENT) != 0ull;
}
DEV bool pend1(v4u f) { return __builtin_amdgcn_ballot_w64(max4(f, 0u) == SENT) != 0ull; }
template <int N>
DEV void settle(v4u (&f)[N], rsrc_t rs, int voff, int& budget) {
  if (pending<N>(f)) {
    bool again;
    do {
      asm volatile("" ::: "memory");
#pragma unroll
      for (int i = 0; i < N; ++i)
        if (pend1(f[i])) f[i] = ld128c(rs, voff + (i & 3) * 1024, (i >> 2) * 4096);
      --budget;
      again = pending<N>(f) & (budget > 0);
    } while (again);
  }
}
DEV float vfma(float a, float b, float c) { return __builtin_fmaf(a, b, c); }
DEV float vfma_s(float a, float sb, float c) { return __builtin_fmaf(a, sb, c); }
DEV float vmul(float a, float b) { return a * b; }
DEV bool all4(v4u f, u32 tag) { return __builtin_amdgcn_ballot_w64(!((f[0] == tag) & (f[1] == tag) & (f[2] == tag) & (f[3] == tag))) == 0ull; }
DEV void wait_flags(v4u f, rsrc_t rs, int voff, u32 tag, int& budget) {
  while (!all4(f, tag) & (budget > 0)) {
    __builtin_amdgcn_s_sleep(1);
    asm volatile("" ::: "memory");
    f = ld128c(rs, voff, 0);
    --budget;
  }
  asm volatile("" ::: "memory");
}
DEV float silu(float x) { return x * __builtin_amdgcn_rcpf(1.f + __builtin_amdgcn_exp2f(-1.44269504f * x)); }

#define STAMP(k) do { } while (0)

__global__ void __launch_bounds__(512, 2) NOPK
fused(const void* __restrict__ px, const void* __restrict__ pwqkv, const void* __restrict__ pwo, const void* __restrict__ pw1,
      const void* __restrict__ pw2, const void* __restrict__ pc, const void* __restrict__ pwm, void* pout, void* pws) {
  extern __shared__ __attribute__((aligned(16))) char smem[];
  LAS char* L = (LAS char*)smem;
  STAMP(0);
  const int tid = threadIdx.x, lane = tid & 63, w = __builtin_amdgcn_readfirstlane(tid >> 6);
  const int c = lane & 15, g = lane >> 4, l16 = lane * 16;
  const int b = blockIdx.x, xcd = b & 7, j = b >> 3, cl = xcd * 4 + (j >> 3), rank = j & 7, t0 = cl * 64;
  int budget = SPIN_CAP;

  u32 codepf;
  {
    u64 pcv;
    u32 clen;
    asm volatile("s_getpc_b64 %0\n.Lfk_pc0:\n\ts_mov_b32 %1, .Lfk_pc1-.Lfk_pc0" : "=s"(pcv), "=s"(clen));
    codepf = ld32(mkrs((const void*)pcv, w == 0 ? (int)clen : 0), lane * 128 + (j & (CODE_SLICES - 1)) * 8192, 0);
  }
  asm volatile("" ::"s"(px), "s"(pwqkv), "s"(pwo), "s"(pw1), "s"(pw2), "s"(pc), "s"(pwm), "s"(pout), "s"(pws));
  const rsrc_t rws = mkrs(pws, WS_END);
  const u32 tag = *(const __attribute__((address_space(4))) u32*)((const char*)pws + WS_EPOCH) + 1u;
  const int rr = (rank + xcd) & 7, pk_ = 4 * rr + (j >> 3);
  const bool packer = rr < 7;
  const int uq = xcd + 8 * (j >> 4), ukh = (j >> 3) & 1;
  const bool prodL = rank == 7 && packer;
  u32 rm[32];
  v4u cf[2];
  const rsrc_t rc = mkrs(pc, D * 2), rwm = mkrs(pwm, D * 6 * D * 2);
  auto arrive = [&]() LAM {
    asm volatile("s_waitcnt lgkmcnt(0)" ::: "memory");
    u32 old = 0;
    if (lane == 0) old = __hip_atomic_fetch_add((LAS u32*)(L + L_CNT), 1u, __ATOMIC_RELAXED, __HIP_MEMORY_SCOPE_WORKGROUP);
    const bool last = __builtin_amdgcn_readfirstlane(old) == 6;
    asm volatile("" ::: "memory");
    return last;
  };
  auto silu8 = [&](v4u x) LAM {
    v4u sf;
#pragma unroll
    for (int i = 0; i < 4; ++i) sf[i] = pk(silu(lo16(x[i])), silu(hi16(x[i])));
    return sf;
  };
  const f32x4 z4 = {0.f, 0.f, 0.f, 0.f};
  const rsrc_t rx = mkrs(px, S * D * 2);
  v4u xr[10];
  int xa[5];
  xa[0] = rank >= 5 ? rank - 5 : rank;
#pragma unroll
  for (int a = 1; a < 5; ++a) xa[a] = xa[a - 1] == 4 ? 0 : xa[a - 1] + 1;
  auto x_load = [&]() LAM {
#pragma unroll
    for (int i = 0; i < 10; ++i) {
      int tok = t0 - 16 + w + 16 * xa[i >> 1] + 8 * (i & 1);
      tok = tok < 0 ? 0 : tok;
      xr[i] = ld128(rx, lane < 56 ? l16 : OOB, tok * 896);
    }
  };
  u32 ra[8], rb[8];
  const int ks = pk_ >> 1;
  if (packer) {
    if (w < 3) {
      const int pr = 3 * (pk_ & 1) + w;
      const rsrc_t rs = mkrs(pwqkv, D * 3 * D * 2);
      const int vo = c < 14 ? g * 8 * 2688 + c * 4 : OOB;
      const int so = ks * 32 * 2688 + (pr >> 1) * 896 + (pr & 1) * 56 + rank * 112;
#pragma unroll
      for (int jj = 0; jj < 8; ++jj) ra[jj] = ld32(rs, vo + (jj & 1) * 2688, so + (jj >> 1) * 5376);
    } else if (w == 3) {
      const int q = rank * 28 + pk_;
      ra[0] = ld32(mkrs(pwo, D * D * 2), lane < 14 ? q * 1792 + lane * 128 : OOB, 0);
      ra[1] = ld32(rwm, lane < 56 ? (2 * q + (lane >= 28)) * 5376 + 1792 + (lane >= 28 ? lane - 28 : lane) * 128 : OOB, 0);
    } else {
      const int jw = 4 * (pk_ & 1) + w - 4;
      const rsrc_t rs = mkrs(pw1, D * F * 2), rw2 = mkrs(pw2, F * D * 2);
      const int v1 = jw < 7 ? g * 4 * 3584 + c * 4 : OOB, s1 = ks * 32 * 3584 + rank * 448 + jw * 64;
#pragma unroll
      for (int jj = 0; jj < 8; ++jj) ra[jj] = ld32(rs, v1 + (jj & 1) * 3584, s1 + ((jj & 2) + (jj >> 2) * 16) * 3584);
      const int vo = rank < 7 ? g * 8 * 896 + rank * 128 + (c >> 2) * 8 + (c & 1) * 4 + (c & 2) * 16 : OOB;
      const int so = (pk_ * 64 + ((w >> 1) & 1) * 32) * 896 + (w & 1) * 64;
#pragma unroll
      for (int jj = 0; jj < 8; ++jj) rb[jj] = ld32(rw2, vo + (jj & 3) * 896, so + (jj >> 2) * 3584);
    }
    if (prodL && tid == 0) *(LAS u32*)(L + L_CNT) = 0u;
    SB();
    x_load();
    SB();
    if (w < 3) {
      v4u f0, f1;
      xpose(ra, f0, f1);
      const int dst = WS_PQ + ((rank * 12 + (3 * (pk_ & 1) + w) * 2) * 14 + ks) * 1024;
      st128c(f0, rws, l16, dst);
      st128c(f1, rws, l16, dst + 14336);
      drain();
    }
    if (w == 3) asm volatile("" ::"v"(ra[0]), "v"(ra[1]));
  } else {
    const int vo = (w < 7 && uq < 14) ? (ukh * 224 + w * 32 + g * 8) * 5376 + uq * 128 + c * 4 : OOB;
#pragma unroll
    for (int i = 0; i < 16; ++i) rm[i] = ld32(rwm, vo + (i >> 3) * 64, (i & 7) * 5376);
    cf[0] = ld128(rc, ukh * 448 + w * 64 + g * 16, 0);
    if (tid == 0) *(LAS u32*)(L + L_CNT) = 0u;
    bar();
    if (w < 7 && uq < 14) {
      const v4u sf = silu8(cf[0]);
#pragma unroll
      for (int hh = 0; hh < 2; ++hh) {
        v4u f0, f1;
        xpose(rm + 8 * hh, f0, f1);
        const f32x4 a0 = mfma(f0, sf, z4), a1 = mfma(f1, sf, z4);
        if (c == 0) {
          stsf4(L, L_MODP + w * 256 + hh * 128 + g * 32, a0);
          stsf4(L, L_MODP + w * 256 + hh * 128 + g * 32 + 16, a1);
        }
      }
      if (arrive()) {
        float s0 = 0.f;
#pragma unroll
        for (int k = 0; k < 7; ++k) s0 += *(LAS float*)(L + L_MODP + k * 256 + (lane >> 3) * 32 + (lane & 1) * 16 + (lane & 6) * 2);
        st64c(v2u{asu(s0), tag}, rws, WS_MU + (uq * 64 + lane) * 16 + ukh * 8, 0);
      }
    }
    x_load();
  }
  if (w < 4) st32c(tag, rws, lane == 0 ? WS_FA + rank * 512 + pk_ * 16 + w * 4 : OOB, 0);
  STAMP(1);
  const int o_cl = WS_O + cl * 65536, x_cl = WS_X + cl * 65536, h_cl = WS_H + cl * H_CL;
#pragma unroll
  for (int i = 0; i < 10; ++i) pin(xr[i]);
  STAMP(2);
  if (prodL) {
    const int vo = (w * 64 + g * 8) * 5376 + 1792 + pk_ * 128 + c * 4;
#pragma unroll
    for (int i = 0; i < 32; ++i) rm[i] = ld32(rwm, vo + ((i >> 3) & 1) * 64, ((i >> 4) * 32 + (i & 7)) * 5376);
    cf[0] = ld128(rc, w * 128 + g * 16, 0);
    cf[1] = ld128(rc, w * 128 + g * 16 + 64, 0);
  }
  if (rank < 7) {
    const u64 wa = (u64)pwo;
    const v4u rso = {(u32)wa, (u32)(wa >> 32) & 0xFFFFu, (u32)(D * D * 2), 0x00020000u};
    const int r8 = lane >> 3, ch = lane & 7;
    const int v0 = r8 * 896 + rank * 128 + ((ch ^ (r8 & 2)) << 4), v1 = v0 ^ 64;
#pragma unroll
    for (int i = 0; i < 7; ++i) dma1kp(rso, ((w + i) & 1) ? v1 : v0, (w * 7 + i) * 7168, L_B + (w * 7 + i) * 1024);
    if (tid < 32) sts128(L, L_B + 57344 + tid * 16, v4u{0u, 0u, 0u, 0u});
  }
  SB();
  f32x2 t[10][4];
  {
    float sm[12], sq[12];
#pragma unroll
    for (int i = 0; i < 10; ++i) sm[i] = sq[i] = 0.f;
#pragma unroll
    for (int e = 0; e < 4; ++e)
#pragma unroll
      for (int i = 0; i < 10; ++i) {
        sm[i] = dot2(xr[i][e], 0x3F803F80u, sm[i]);
        sq[i] = dot2(xr[i][e], xr[i][e], sq[i]);
      }
    sm[10] = sm[11] = sq[10] = sq[11] = 0.f;
    float rs_[3], nn[3];
#pragma unroll
    for (int q = 0; q < 3; ++q) {
      const float m = rowsum(fold16(fold32(sm[4 * q], sm[4 * q + 2]), fold32(sm[4 * q + 1], sm[4 * q + 3]))) * (1.f / D);
      const float v = rowsum(fold16(fold32(sq[4 * q], sq[4 * q + 2]), fold32(sq[4 * q + 1], sq[4 * q + 3]))) * (1.f / D);
      rs_[q] = __builtin_amdgcn_rsqf(fmaxf(v - m * m, 0.f) + 1e-5f);
      nn[q] = -m * rs_[q];
      if (c == 0 && 4 * q + g < 10) *(LAS float*)(L + L_MU1 + (w + 16 * (g < 2 ? xa[2 * q] : xa[q < 2 ? 2 * q + 1 : 0]) + 8 * (g & 1)) * 4) = m;
    }
#pragma unroll
    for (int i = 0; i < 10; ++i) {
      const float r = asf(__builtin_amdgcn_readlane(asu(rs_[i >> 2]), 16 * (i & 3)));
      float nv = asf(__builtin_amdgcn_readlane(asu(nn[i >> 2]), 16 * (i & 3)));
      asm("" : "+v"(nv));
#pragma unroll
      for (int e = 0; e < 4; ++e) {
        t[i][e] = fma2(up2(xr[i][e]), f32x2{r, r}, f32x2{nv, nv});
        asm volatile("" : "+v"(t[i][e]));
      }
    }
  }
  asm volatile("" ::"v"(codepf));
  STAMP(3);
  if (w >= 4) {
    if (packer) {
      v4u f0, f1;
      xpose(ra, f0, f1);
      const int jw = 4 * (pk_ & 1) + w - 4, d1 = WS_P1 + rank * 200704 + (jw * 14 + ks) * 2048;
      st128c(f0, rws, jw < 7 ? l16 : OOB, d1);
      st128c(f1, rws, jw < 7 ? l16 + 1024 : OOB, d1);
      xpose(rb, f0, f1);
      const int d2 = WS_P2 + rank * 229376 + (pk_ * 4 + w - 4) * 2048;
      st128c(f0, rws, rank < 7 ? l16 : OOB, d2);
      st128c(f1, rws, rank < 7 ? l16 + 1024 : OOB, d2);
    }
    const v4u sv = {SENT, SENT, SENT, SENT};
    const int r = w - 4;
    st128c(sv, rws, l16, o_cl + (r * 16 + 2 * rank) * 1024);
    st128c(sv, rws, l16 + 1024, o_cl + (r * 16 + 2 * rank) * 1024);
    if (rank < 7) {
      st128c(sv, rws, l16, x_cl + (r * 14 + 2 * rank) * 1024);
      st128c(sv, rws, l16 + 1024, x_cl + (r * 14 + 2 * rank) * 1024);
      st128c(sv, rws, g == 0 ? l16 : OOB, x_cl + XS_OFF + (r * 7 + rank) * 256);
    }
  } else {
    v4u m[4];
    const int mo = WS_MU + w * 3584 + (lane < 56 ? lane : 55) * 64;
    for (;;) {
      asm volatile("" ::: "memory");
#pragma unroll
      for (int i = 0; i < 4; ++i) m[i] = ld128c(rws, mo + i * 16, 0);
      bool ok = true;
#pragma unroll
      for (int i = 0; i < 4; ++i) ok = ok & (m[i][1] == tag) & (m[i][3] == tag);
      --budget;
      if ((__builtin_amdgcn_ballot_w64(!ok) == 0ull) | (budget <= 0)) break;
      __builtin_amdgcn_s_sleep(2);
    }
    u32 p0 = pk(asf(m[0][0]) + asf(m[0][2]), asf(m[1][0]) + asf(m[1][2])), p1 = pk(asf(m[2][0]) + asf(m[2][2]), asf(m[3][0]) + asf(m[3][2]));
    if (w >= 2) {
      p0 = pk(1.f + lo16(p0), 1.f + hi16(p0));
      p1 = pk(1.f + lo16(p1), 1.f + hi16(p1));
    }
    if (lane < 56) stsf4(L, L_PAIR + w * 896 + l16, f32x4{lo16(p0), hi16(p0), lo16(p1), hi16(p1)});
  }
  bar();
  STAMP(4);
  const int fao = w == 2 ? WS_FA + rank * 512 + (lane & 31) * 16 : OOB;
  const v4u fa = ld128c(rws, fao, 0);
  SB();
  if (lane < 56) {
    const f32x4 ca[2] = {ldsf4(L, L_PAIR + 1792 + 2 * l16), ldsf4(L, L_PAIR + 1808 + 2 * l16)}, sa[2] = {ldsf4(L, L_PAIR + 2 * l16), ldsf4(L, L_PAIR + 16 + 2 * l16)};
    const int base = L_A + ((lane >> 2) * 64 + (lane & 3) * 16 + (w ^ (lane & 7))) * 16;
#pragma unroll
    for (int i = 0; i < 10; ++i) {
      v4u hv;
      hv[0] = pk2(fma2(t[i][0], ca[0].xy, sa[0].xy));
      hv[1] = pk2(fma2(t[i][1], ca[0].zw, sa[0].zw));
      hv[2] = pk2(fma2(t[i][2], ca[1].xy, sa[1].xy));
      hv[3] = pk2(fma2(t[i][3], ca[1].zw, sa[1].zw));
      sts128(L, base + xa[i >> 1] * 14336 + (i & 1) * 128, hv);
    }
  }
  if (prodL && w < 7) {
    const v4u sf0 = silu8(cf[0]), sf1 = silu8(cf[1]);
#pragma unroll
    for (int hh = 0; hh < 2; ++hh) {
      v4u f0, f1;
      xpose(rm + 8 * hh, f0, f1);
      f32x4 a0 = mfma(f0, sf0, z4), a1 = mfma(f1, sf0, z4);
      xpose(rm + 16 + 8 * hh, f0, f1);
      a0 = mfma(f0, sf1, a0);
      a1 = mfma(f1, sf1, a1);
      if (c == 0) {
        stsf4(L, L_MODP + w * 256 + hh * 128 + g * 32, a0);
        stsf4(L, L_MODP + w * 256 + hh * 128 + g * 32 + 16, a1);
      }
    }
    if (arrive()) {
      float s0 = 0.f, s1 = 0.f;
#pragma unroll
      for (int k = 0; k < 7; ++k) {
        s0 += *(LAS float*)(L + L_MODP + k * 256 + ((lane >> 2) & 7) * 32 + (lane & 3) * 4);
        s1 += *(LAS float*)(L + L_MODP + k * 256 + ((lane >> 2) & 7) * 32 + (lane & 3) * 4 + 16);
      }
      st64c(v2u{pkn(s0, s1), tag}, rws, lane < 32 ? WS_MOD + (pk_ * 32 + lane) * 8 : OOB, 0);
    }
  }
  drain();
  if (w == 2) wait_flags(fa, rws, fao, tag, budget);
  if (w >= 4) st32c(tag, rws, lane == 0 ? WS_FB + rank * 512 + cl * 16 + (w - 4) * 4 : OOB, 0);
  bar();
  STAMP(5);
  const int kh = w >> 2;
  v4u wqf[21];
  const int pe = WS_PQ + ((rank * 12 + 4 + w) * 14 + 7 * kh) * 1024, po = WS_PQ + ((rank * 12 + 4 + w) * 14 + 7 - 7 * kh) * 1024;
  const int pq = WS_PQ + ((rank * 12 + (w & 3)) * 14 + 7 * kh) * 1024;
  auto wq_load = [&](int n) LAM {
    const int i = n / 3, k = n - 3 * i;
    wqf[n] = ld128c(rws, l16 + (i & 3) * 1024, (k == 0 ? pe : k == 1 ? pq : po) + (i >> 2) * 4096);
  };
#pragma unroll
  for (int n = 0; n < NQW; ++n) wq_load(n);
  SB();

  f32x4 akv[5], aq[4];
  const int fbo = w == 0 ? WS_FB + (lane < 32 ? rank * 512 + lane * 16 : (lane & 7) * 512 + cl * 16) : OOB;
  {
    const int hb0 = L_A + (g * 16 + (c ^ g)) * 16, hb1 = L_A + (g * 16 + (c ^ (4 + g))) * 16;
    const int ad[2][2] = {{(kh ? hb1 : hb0) + kh * 7168, (kh ? hb0 : hb1) + kh * 7168}, {(kh ? hb0 : hb1) + (1 - kh) * 7168, (kh ? hb1 : hb0) + (1 - kh) * 7168}};
#pragma unroll
    for (int r = 0; r < 5; ++r) akv[r] = f32x4{0.f, 0.f, 0.f, 0.f};
#pragma unroll
    for (int r = 0; r < 4; ++r) aq[r] = f32x4{0.f, 0.f, 0.f, 0.f};
    v4u hf[5], fb, mm[7];
    const int mmo = w == 1 ? l16 : OOB;
#pragma unroll
    for (int r = 0; r < 5; ++r) hf[r] = lds128(L, ad[0][0] + r * 14336);
    SB();
#pragma unroll
    for (int s = 0; s < 14; ++s) {
#pragma unroll
      for (int r = 0; r < 5; ++r) {
        akv[r] = mfma(wqf[3 * (s >> 1) + 2 * (s & 1)], hf[r], akv[r]);
        if (!(s & 1) && r > 0) aq[r - 1] = mfma(wqf[3 * (s >> 1) + 1], hf[r], aq[r - 1]);
        if (s < 13) hf[r] = lds128(L, ad[(s + 1) & 1][((s + 1) >> 1) & 1] + r * 14336 + ((s + 1) >> 1) * 1024);
        if (r == 0 && NQW + 3 * (s >> 1) + 2 * (s & 1) < 21) wq_load(NQW + 3 * (s >> 1) + 2 * (s & 1));
        if (r == 3 && !(s & 1) && NQW + 3 * (s >> 1) + 1 < 21) wq_load(NQW + 3 * (s >> 1) + 1);
        if (s == 11 && r == 0) fb = ld128c(rws, fbo, 0);
        if (s == 10 && r == 0) {
#pragma unroll
          for (int i = 0; i < 7; ++i) mm[i] = ld128c(rws, mmo + (i & 3) * 1024, WS_MOD + (i >> 2) * 4096);
        }
        SB();
      }
    }
    if (w == 0) wait_flags(fb, rws, fbo, tag, budget);
    if (w == 1) {
      for (;;) {
        bool ok = true;
#pragma unroll
        for (int i = 0; i < 7; ++i) ok = ok & (mm[i][1] == tag) & (mm[i][3] == tag);
        --budget;
        if ((__builtin_amdgcn_ballot_w64(!ok) == 0ull) | (budget <= 0)) break;
        asm volatile("" ::: "memory");
#pragma unroll
        for (int i = 0; i < 7; ++i) mm[i] = ld128c(rws, mmo + (i & 3) * 1024, WS_MOD + (i >> 2) * 4096);
      }
      const int la = L_GA + (lane & ~7) * 8 + (lane & 3) * 16 + (lane & 4) * 2;
#pragma unroll
      for (int i = 0; i < 7; ++i) {
        const int q = i * 64 + lane;
        const float one = (q >= 224 && q < 336) ? 1.f : 0.f;
        const v2u o = {pk(one + lo16(mm[i][0]), one + hi16(mm[i][0])), pk(one + lo16(mm[i][2]), one + hi16(mm[i][2]))};
        sts64(L, la + i * 512, o);
      }
    }
  }
  const u64 wsa = (u64)pws;
  const v4u rsw = {(u32)wsa, (u32)(wsa >> 32) & 0xFFFFu, rank < 7 ? (u32)WS_END : 0u, 0x00020000u};
  STAMP(6);
  bar();
  {
    const int qb = L_A + ((w >> 1) & 1) * QK_HD + l16;
#pragma unroll
    for (int r = 0; r < 5; ++r) sts64(L, qb + (kh * 5 + r) * 1024 + (w & 1) * 8, v2u{pk(akv[r][0], akv[r][1]), pk(akv[r][2], akv[r][3])});
#pragma unroll
    for (int r = 0; r < 4; ++r) stsf4(L, qb + (10 + kh * 8 + 2 * r + (w & 1)) * 1024, aq[r]);
  }
  bar();
  {
    const int rt = w >> 1, hd = w & 1;
    const int qb = L_A + hd * QK_HD + l16 + rt * 1024;
    const int qq = L_A + hd * QK_HD + l16 + (10 + 2 * rt) * 1024;
    const f32x4 qa = ldsf4(L, qq) + ldsf4(L, qq + 8192), qc = ldsf4(L, qq + 1024) + ldsf4(L, qq + 9216);
    const v4u k0 = lds128(L, qb), k1 = lds128(L, qb + 1024);
    const int vb = L_A + hd * QK_HD + (5 + rt) * 1024 + ((c & 3) * 16 + 4 * g + (c >> 2)) * 16;
    const v2u v00 = trread(L, vb), v01 = trread(L, vb + 8), v10 = trread(L, vb + 1024), v11 = trread(L, vb + 1032);
    const v4u qf = {pk(qa[0], qa[1]), pk(qa[2], qa[3]), pk(qc[0], qc[1]), pk(qc[2], qc[3])};
    const f32x4 z = {0.f, 0.f, 0.f, 0.f};
    const f32x4 s0 = mfma(k0, qf, z), s1 = mfma(k1, qf, z);
    const int tq = t0 + 16 * rt + c;
    float sv[8];
    float mx = -3.0e38f;
#pragma unroll
    for (int e = 0; e < 8; ++e) {
      const int delta = c + (e < 4 ? 16 : 0) - 4 * g - (e & 3);
      const bool ok = (delta & ~14) == 0 && delta <= tq;
      sv[e] = ok ? (e < 4 ? s0[e & 3] : s1[e & 3]) * (0.18898223650461362f * 1.44269504088896341f) : -3.0e38f;
      mx = fmaxf(mx, sv[e]);
    }
    mx = gmax(mx);
    float sum = 0.f;
#pragma unroll
    for (int e = 0; e < 8; ++e) {
      sv[e] = __builtin_amdgcn_exp2f(sv[e] - mx);
      sum += sv[e];
    }
    const float inv = __builtin_amdgcn_rcpf(gsum(sum));
    v4u pf;
#pragma unroll
    for (int i = 0; i < 4; ++i) pf[i] = pk(sv[2 * i] * inv, sv[2 * i + 1] * inv);
    const f32x4 o0 = mfma(v4u{v00[0], v00[1], v10[0], v10[1]}, pf, z), o1 = mfma(v4u{v01[0], v01[1], v11[0], v11[1]}, pf, z);
    v4u of;
#pragma unroll
    for (int i = 0; i < 4; ++i) of[i] = canon(pk(o0[i], o1[i]));
    st128c(of, rws, l16, o_cl + (rt * 16 + 2 * rank + hd) * 1024);
  }
  STAMP(7);
  bar();
  v4u w1f[28];
  const int w1o = WS_P1 + rank * 200704 + w * 28672, w1v = w < 7 ? l16 : OOB;
  auto w1_load = [&](int n) LAM { w1f[n] = ld128c(rws, w1v + (n & 3) * 1024, w1o + (n >> 2) * 4096); };
#pragma unroll
  for (int n = 0; n < N1B; ++n) w1_load(n);
  SB();
  const int rt = w & 3;
  v4u x1p = {0u, 0u, 0u, 0u};
  if (rank < 7) {
    const int xo = c * 896 + g * 8 + (t0 + 16 * rt) * 896 + rank * 128 + kh * 64;
    const v2u xr0 = ld64(rx, xo, 0), xr1 = ld64(rx, xo + 32, 0);
    int wb[4];
#pragma unroll
    for (int x = 0; x < 4; ++x) wb[x] = L_B + (kh * 224 + 8 * g + (c >> 2)) * 128 + ((x ^ ((c >> 3) | ((g & 1) << 1))) << 5) + (c & 3) * 8;
    auto wo_read = [&](int q) LAM {
      const int hs = q >> 2, ct = q & 3;
      const v2u lo = trread(L, wb[ct ^ (((7 * hs) >> 1) & 1) * 2] + 28 * hs * 128);
      const v2u hi = trread(L, wb[ct ^ (((7 * hs + 1) >> 1) & 1) * 2] + (28 * hs + 4) * 128);
      return v4u{lo[0], lo[1], hi[0], hi[1]};
    };
    v4u wo[16];
#pragma unroll
    for (int q = 0; q < 16; ++q) wo[q] = wo_read(q);
    const int vo = rank * 128 + kh * 64 + g * 16;
    const v4u ga = lds128(L, L_GA + vo), cb = lds128(L, L_CB + vo);
    const float mu1 = *(LAS float*)(L + L_MU1 + (16 + 16 * rt + c) * 4);
    v4u of[8];
    const int ovo = o_cl + (rt * 16 + 8 * kh) * 1024 + l16;
    issue<8>(of, rws, ovo);
    SB();
    settle<8>(of, rws, ovo, budget);
    STAMP(8);
    f32x4 acc[4];
#pragma unroll
    for (int q = 0; q < 4; ++q) acc[q] = f32x4{0.f, 0.f, 0.f, 0.f};
#pragma unroll
    for (int q = 0; q < 32; ++q) {
      acc[q & 3] = mfma(wo[q & 15], of[q >> 2], acc[q & 3]);
      if (q < 16) wo[q] = wo_read(16 + q);
      if ((q & 1) && N1B + (q >> 1) < N1C) w1_load(N1B + (q >> 1));
      SB();
    }
#pragma unroll
    for (int q = 0; q < 4; ++q) stsf4(L, L_A + (w * 4 + q) * 1024 + l16, acc[q]);
    bar();
    const int pb = L_A + (rt * 4 + 2 * kh) * 1024 + l16;
    const f32x4 a0 = ldsf4(L, pb) + ldsf4(L, pb + 16384), a1 = ldsf4(L, pb + 1024) + ldsf4(L, pb + 17408);
    const v4u xres = {xr0[0], xr0[1], xr1[0], xr1[1]};
    v4u xp;
    f32x2 s1v = {0.f, 0.f}, s2v = {0.f, 0.f};
#pragma unroll
    for (int i = 0; i < 4; ++i) {
      const u32 ao = pk2(i == 0 ? a0.xy : i == 1 ? a0.zw : i == 2 ? a1.xy : a1.zw);
      const u32 y = pk2(up2(ga[i]) * up2(ao));
      x1p[i] = pk2(up2(xres[i]) + up2(y));
      const f32x2 d = up2(x1p[i]) - f32x2{mu1, mu1};
      s1v += d;
      s2v = fma2(d, d, s2v);
      xp[i] = canon(pk2(d * up2(cb[i])));
    }
    float s1 = s1v.x + s1v.y, s2 = s2v.x + s2v.y;
    st128c(xp, rws, l16, x_cl + (rt * 14 + 2 * rank + kh) * 1024);
    s1 = gsum(s1);
    s2 = gsum(s2);
    st64c(v2u{canon(asu(s1)), canon(asu(s2))}, rws, g == 0 ? l16 : OOB, x_cl + XS_OFF + (rt * 7 + rank) * 256 + kh * 8);
  } else {
#pragma unroll
    for (int n = N1B; n < N1C; ++n) w1_load(n);
    bar();
  }
  STAMP(9);
  bar();
#pragma unroll
  for (int n = N1C; n < 28; ++n) w1_load(n);
  v4u w2f[28];
  const int w2o = WS_P2 + rank * 229376 + w * 28672, w2v = rank < 7 ? l16 : OOB;
  auto w2_load = [&](int n) LAM { w2f[n] = ld128c(rws, w2v + (n & 3) * 1024, w2o + (n >> 2) * 4096); };
  constexpr int L_W2 = 57344, NST = 28 - N2B;
  static_assert(L_W2 + 8 * NST * 1024 <= L_GA, "W2 staging");
  auto w2_dma = [&](int n) LAM { dma1k(rsw, l16, w2o + (N2B + n) * 1024, L_W2 + (w * NST + n) * 1024); };
  {
    v4u xf[7], xs[1];
    const int pr = 4 * kh + g;
    const int xo = x_cl + (rt * 14 + 7 * kh) * 1024 + l16, so = pr < 7 ? x_cl + XS_OFF + (rt * 7 + pr) * 256 + c * 16 : OOB;
    issue<7>(xf, rws, xo);
    issue<1>(xs, rws, so);
    SB();
    {
      f32x4 u0 = {0.f, 0.f, 0.f, 0.f}, u1 = {0.f, 0.f, 0.f, 0.f};
      const int vb = ((c & 1) ? L_SB : L_CB) + g * 16;
      v4u bfv[14];
#pragma unroll
      for (int s = 0; s < 14; ++s) bfv[s] = lds128(L, vb + s * 64);
      SB();
#pragma unroll
      for (int s = 0; s < 14; ++s) {
        u0 = mfma(w1f[2 * s], bfv[s], u0);
        u1 = mfma(w1f[2 * s + 1], bfv[s], u1);
      }
      const float sc = (c & 1) ? -1.44269504f : 1.f;
      if (c < 2 && w < 7) {
        stsf4(L, L_UV + w * 256 + g * 64 + c * 32, u0 * sc);
        stsf4(L, L_UV + w * 256 + g * 64 + c * 32 + 16, u1 * sc);
      }
    }
    SB();
    settle<7>(xf, rws, xo, budget);
#pragma unroll
    for (int i = 0; i < 7; ++i) sts128(L, L_A + (rt * 14 + 7 * kh + i) * 1024 + l16, xf[i]);
    settle<1>(xs, rws, so, budget);
    const float s1 = gsum(asf(xs[0][0]) + asf(xs[0][2])), s2 = gsum(asf(xs[0][1]) + asf(xs[0][3]));
    if (g == 0) sts64(L, L_ST + ((rt * 2 + kh) * 16 + c) * 8, v2u{asu(s1), asu(s2)});
  }
  STAMP(10);
  bar();
  STAMP(11);
  const int fho = lane == 0 ? WS_FH + cl * 1024 + rank * 128 + w * 4 : OOB;
  {
    v4u xf[4];
#pragma unroll
    for (int i = 0; i < 4; ++i) xf[i] = lds128(L, L_A + i * 1024 + l16);
    const int ub = L_UV + (w < 7 ? w : 0) * 256 + g * 64;
    const f32x4 uu[2] = {ldsf4(L, ub), ldsf4(L, ub + 16)}, vv[2] = {ldsf4(L, ub + 32), ldsf4(L, ub + 48)};
    float nmu[4], rsl[4];
#pragma unroll
    for (int r = 0; r < 4; ++r) {
      const v2u sta = *(LAS v2u*)(L + L_ST + (r * 32 + c) * 8), stb = *(LAS v2u*)(L + L_ST + (r * 32 + 16 + c) * 8);
      const float mu = (asf(sta[0]) + asf(stb[0])) * (1.f / D);
      const float var = fmaxf((asf(sta[1]) + asf(stb[1])) * (1.f / D) - mu * mu, 0.f);
      nmu[r] = -mu;
      rsl[r] = -1.44269504f * __builtin_amdgcn_rsqf(var + 1e-5f);
    }
    SB();
    f32x4 a[2][2];
    float y0;
    v4u hf;
    const float nl2e = -1.44269504f;
    auto epi = [&](int r, int e) LAM {
      const float z = vfma(vfma(nmu[r], uu[e & 1][e >> 1], a[r & 1][e & 1][e >> 1]), rsl[r], vv[e & 1][e >> 1]);
      const float y = z * __builtin_amdgcn_rcpf(vfma(__builtin_amdgcn_exp2f(z), nl2e, nl2e));
      if (e & 1) hf[e >> 1] = pkn(y0, y);
      else y0 = y;
    };
    auto publish = [&](int r) LAM {
      st128c(hf, rws, w1v, h_cl + (rank * 28 + w + r * 7) * 1024);
    };
#pragma unroll
    for (int r = 0; r < 4; ++r) {
      a[r & 1][0] = a[r & 1][1] = f32x4{0.f, 0.f, 0.f, 0.f};
#pragma unroll
      for (int s = 0; s < 14; ++s) {
        const int n = r * 14 + s;
        a[r & 1][0] = mfma(w1f[2 * s], xf[n & 3], a[r & 1][0]);
        a[r & 1][1] = mfma(w1f[2 * s + 1], xf[n & 3], a[r & 1][1]);
        if (n + 4 < 56) xf[n & 3] = lds128(L, L_A + (n + 4) * 1024 + l16);
        if (r > 0 && s >= 1 && s < 9) epi(r - 1, s - 1);
        if (r > 1 && s == 8) { drain(); st32c(tag, rws, fho, (r - 2) * 32); }
        if (r > 0 && s == 9) publish(r - 1);
        const int slot = r == 0 ? s : r == 1 ? (s < 7 ? 14 + s : s >= 10 ? 11 + s : 99) : r == 2 ? (s < 3 ? 25 + s : 99) : 99;
        if (slot < N2B) w2_load(slot);
        else if (slot < 28) w2_dma(slot - N2B);
        SB();
      }
    }
#pragma unroll
    for (int e = 0; e < 8; ++e) epi(3, e);
    drain();
    st32c(tag, rws, fho, 64);
    publish(3);
    if (rank == 7) {
      drain();
      st32c(tag, rws, fho, 96);
    }
  }
  STAMP(12);
  if (rank < 7) {
    const int fwo = lane < 32 ? WS_FH + cl * 1024 + w * 128 + lane * 4 : OOB;
    u32 fh = ld32c(rws, fwo, 0);
#pragma unroll
    for (int n = 0; n < NST; ++n) w2f[N2B + n] = lds128(L, L_W2 + (w * NST + n) * 1024 + l16);
    drain();
    st32c(tag, rws, fho, 96);
    bar();
    v4u hf[4][7];
    const int ho = h_cl + w * 28672 + l16;
    auto need = [&](u64 mask) LAM {
      while (((__builtin_amdgcn_ballot_w64(fh != tag) & mask) != 0ull) & (budget > 0)) {
        asm volatile("" ::: "memory");
        fh = ld32c(rws, fwo, 0);
        --budget;
      }
      asm volatile("" ::: "memory");
    };
    need(0x7F7Full);
    issue<7>(hf[0], rws, ho);
    issue<7>(hf[1], rws, ho + 7168);
    SB();
#pragma unroll
    for (int r = 0; r < 4; ++r) {
      if (r < 2) need(0x7Full << (16 + 8 * r));
      f32x4 acc[4];
#pragma unroll
      for (int q = 0; q < 4; ++q) acc[q] = f32x4{0.f, 0.f, 0.f, 0.f};
#pragma unroll
      for (int s = 0; s < 7; ++s) {
#pragma unroll
        for (int q = 0; q < 4; ++q) acc[q] = mfma(w2f[4 * s + q], hf[r][s], acc[q]);
        if (r < 2) hf[r + 2][s] = ld128c(rws, ho + (r + 2) * 7168 + (s & 3) * 1024, (s >> 2) * 4096);
        if (r == 0 && s == 6) fh = ld32c(rws, fwo, 0);
        SB();
      }
#pragma unroll
      for (int q = 0; q < 4; ++q) stsf4(L, ((w * 4 + r) * 4 + q) * 1024 + l16, acc[q]);
      SB();
    }
    STAMP(13);
    const v4u gb = lds128(L, L_GB + rank * 128 + kh * 64 + g * 16);
    bar();
    f32x4 pa[8][2];
#pragma unroll
    for (int p = 0; p < 8; ++p) {
      pa[p][0] = ldsf4(L, ((p * 4 + rt) * 4 + 2 * kh) * 1024 + l16);
      pa[p][1] = ldsf4(L, ((p * 4 + rt) * 4 + 2 * kh + 1) * 1024 + l16);
    }
    SB();
    f32x4 a0 = pa[0][0], a1 = pa[0][1];
#pragma unroll
    for (int p = 1; p < 8; ++p) {
      a0 += pa[p][0];
      a1 += pa[p][1];
    }
    v4u ov;
#pragma unroll
    for (int i = 0; i < 4; ++i) {
      const u32 mo = pk(a0[i], a1[i]);
      const u32 y = pk(lo16(gb[i]) * lo16(mo), hi16(gb[i]) * hi16(mo));
      ov[i] = pk(lo16(x1p[i]) + lo16(y), hi16(x1p[i]) + hi16(y));
    }
    const rsrc_t rout = mkrs(pout, S * D * 2);
    const int oo = c * 896 + g * 8 + (t0 + 16 * rt) * 896 + rank * 128 + kh * 64;
    __builtin_amdgcn_raw_buffer_store_b64(v2u{ov[0], ov[1]}, rout, oo, 0, 0);
    __builtin_amdgcn_raw_buffer_store_b64(v2u{ov[2], ov[3]}, rout, oo + 32, 0, 0);
  } else if (cl == 0 && w == 7) {
    for (;;) {
      asm volatile("" ::: "memory");
      bool ok = true;
#pragma unroll
      for (int i = 0; i < 8; ++i) {
        const v4u f = ld128c(rws, l16 + (i & 3) * 1024, WS_FA + (i >> 2) * 4096);
        ok = ok & (f[0] == tag) & (f[1] == tag) & (f[2] == tag) & (f[3] == tag);
      }
      --budget;
      if ((__builtin_amdgcn_ballot_w64(!ok) == 0ull) | (budget <= 0)) break;
      __builtin_amdgcn_s_sleep(8);
    }
    if (lane == 0) st32c(tag, rws, WS_EPOCH, 0);
  }
  STAMP(14);
  asm volatile(".Lfk_pc1:");
}
}

extern "C" void kernel_launch(void* const* d_in, const int* in_sizes, int n_in, void* d_out, int out_size, void* d_ws,
                              size_t ws_size, hipStream_t stream) {
  (void)hipFuncSetAttribute((const void*)fk::fused, hipFuncAttributeMaxDynamicSharedMemorySize, fk::L_END);
  fk::fused<<<256, 512, fk::L_END, stream>>>(d_in[0], d_in[1], d_in[2], d_in[4], d_in[5], d_in[6], d_in[7], d_out, d_ws);
}
```

```cpp
#include <hip/hip_runtime.h>

typedef unsigned u32;
typedef unsigned long long u64;
typedef u32 v4u __attribute__((ext_vector_type(4)));
typedef u32 v2u __attribute__((ext_vector_type(2)));
typedef float f32x4 __attribute__((ext_vector_type(4)));
typedef float f32x2 __attribute__((ext_vector_type(2)));
typedef short bf16x8 __attribute__((ext_vector_type(8)));
typedef __bf16 bf16x2 __attribute__((ext_vector_type(2)));
typedef __amdgpu_buffer_rsrc_t rsrc_t;
#define LAS __attribute__((address_space(3)))
#define SB() __builtin_amdgcn_sched_barrier(0)
#define NOPK
#define DEV __device__ __forceinline__ NOPK
#define LAM __attribute__((always_inline)) NOPK
#define fmaxf __builtin_fmaxf

namespace fk {
constexpr int S = 2048, D = 448, F = 1792;
constexpr int WS_EPOCH = 0;
constexpr int WS_FA = 81920;
constexpr int WS_FB = 8192;
constexpr int WS_FH = 32768;
constexpr int WS_MOD = 16384;
constexpr int WS_MU = 65536;
constexpr int WS_O = 131072;
constexpr int WS_X = WS_O + 32 * 65536;
constexpr int XS_OFF = 57344;
constexpr int WS_H = WS_X + 32 * 65536;
constexpr int H_CL = 8 * 4 * 7 * 1024;
constexpr int WS_PQ = WS_H + 32 * H_CL;
constexpr int WS_PO = WS_PQ + 8 * 168 * 1024;
constexpr int WS_P2 = WS_PO + 8 * 65536;
constexpr int WS_P1 = WS_P2 + 8 * 224 * 1024;
constexpr int WS_END = WS_P1 + 8 * 196 * 1024;
constexpr int L_A = 0;
constexpr int L_B = 71680;
constexpr int L_PAIR = 137216;
constexpr int L_MODP = 141312;
constexpr int L_SC = 143360;
constexpr int L_MU1 = 154496;
constexpr int L_GA = 155648, L_SB = L_GA + 896, L_CB = L_SB + 896, L_GB = L_CB + 896;
constexpr int L_ST = L_GB + 896;
constexpr int L_UV = L_ST + 1024;
constexpr int L_CNT = L_UV + 1792;
constexpr int L_DUMMY = L_CNT + 16;
constexpr int L_END = L_DUMMY + 1024;
static_assert(L_END <= 163840, "LDS");
constexpr int QK_HD = 26 * 1024;
constexpr u32 SENT = 0xFFFFFFFFu;
constexpr int SPIN_CAP = 1 << 16;
constexpr int OOB = 0x40000000;
constexpr int CODE_SLICES = 4;
#define PSLEEP 6
#define NQW 4
#define N1B 12
#define N1C 22
#define N2A 8
#define N2B 16

DEV float asf(u32 x) { return __builtin_bit_cast(float, x); }
DEV u32 asu(float x) { return __builtin_bit_cast(u32, x); }
DEV float lo16(u32 x) { return asf(x << 16); }
DEV float hi16(u32 x) { return asf(x & 0xFFFF0000u); }
DEV u32 pk(float a, float b) {
  f32x2 v = {a, b};
  return __builtin_bit_cast(u32, __builtin_convertvector(v, bf16x2));
}
DEV u32 pk2(f32x2 v) { return __builtin_bit_cast(u32, __builtin_convertvector(v, bf16x2)); }
DEV u32 pkn(float a, float b) {
  asm("" : "+v"(a));
  asm("" : "+v"(b));
  return pk(a, b);
}
DEV f32x2 up2(u32 x) { return f32x2{lo16(x), hi16(x)}; }
DEV f32x2 fma2(f32x2 a, f32x2 b, f32x2 c) { return __builtin_elementwise_fma(a, b, c); }
DEV u32 canon(u32 v) { return v < 0xFFFFFFFEu ? v : 0xFFFFFFFEu; }
DEV f32x4 mfma(v4u a, v4u b, f32x4 c) {
  return __builtin_amdgcn_mfma_f32_16x16x32_bf16(__builtin_bit_cast(bf16x8, a), __builtin_bit_cast(bf16x8, b), c, 0, 0, 0);
}
DEV float dot2(u32 a, u32 b, float c) {
  return __builtin_amdgcn_fdot2_f32_bf16(__builtin_bit_cast(bf16x2, a), __builtin_bit_cast(bf16x2, b), c, false);
}
DEV rsrc_t mkrs(const void* p, int bytes) { return __builtin_amdgcn_make_buffer_rsrc((void*)p, 0, bytes, 0x00020000); }
DEV u32 ld32(rsrc_t r, int v, int s) { return __builtin_amdgcn_raw_buffer_load_b32(r, v, s, 0); }
DEV v4u ld128(rsrc_t r, int v, int s) { return __builtin_amdgcn_raw_buffer_load_b128(r, v, s, 0); }
DEV u32 ld16(rsrc_t r, int v, int s) { return __builtin_amdgcn_raw_buffer_load_b16(r, v, s, 0); }
DEV v2u ld64(rsrc_t r, int v, int s) { return __builtin_amdgcn_raw_buffer_load_b64(r, v, s, 0); }
DEV v2u ld64c(rsrc_t r, int v, int s) { return __builtin_amdgcn_raw_buffer_load_b64(r, v, s, 16); }
DEV u32 ld32c(rsrc_t r, int v, int s) { return __builtin_amdgcn_raw_buffer_load_b32(r, v, s, 16); }
DEV v4u ld128c(rsrc_t r, int v, int s) { return __builtin_amdgcn_raw_buffer_load_b128(r, v, s, 16); }
DEV void st128c(v4u x, rsrc_t r, int v, int s) { __builtin_amdgcn_raw_buffer_store_b128(x, r, v + s, 0, 16); }
DEV void st64c(v2u x, rsrc_t r, int v, int s) { __builtin_amdgcn_raw_buffer_store_b64(x, r, v, s, 16); }
DEV void st32c(u32 x, rsrc_t r, int v, int s) { __builtin_amdgcn_raw_buffer_store_b32(x, r, v, s, 16); }
DEV v4u lds128(LAS char* L, int off) { return *(LAS v4u*)(L + off); }
DEV f32x4 ldsf4(LAS char* L, int off) { return *(LAS f32x4*)(L + off); }
DEV void sts128(LAS char* L, int off, v4u x) { *(LAS v4u*)(L + off) = x; }
DEV void stsf4(LAS char* L, int off, f32x4 x) { *(LAS f32x4*)(L + off) = x; }
DEV void sts64(LAS char* L, int off, v2u x) { *(LAS v2u*)(L + off) = x; }
typedef short v4s __attribute__((ext_vector_type(4)));
DEV v2u trread(LAS char* L, int off) { return __builtin_bit_cast(v2u, __builtin_amdgcn_ds_read_tr16_b64_v4i16((LAS v4s*)(L + off))); }
DEV void bar() {
  asm volatile("s_waitcnt lgkmcnt(0)" ::: "memory");
  __builtin_amdgcn_s_barrier();
  asm volatile("" ::: "memory");
}
DEV void drain() { asm volatile("s_waitcnt vmcnt(0)" ::: "memory"); }
template <int N>
DEV void waitvm() { asm volatile("s_waitcnt vmcnt(%0)" ::"n"(N) : "memory"); }
DEV void pin(v4u& x) { asm volatile("" : "+v"(x)); }
DEV void dma1k(v4u rs, int voff, int soff, int lds) {
  asm volatile("s_mov_b32 m0, %2\n\ts_nop 0\n\tbuffer_load_dwordx4 %0, %1, %3 offen sc1 lds" ::"v"(voff), "s"(rs), "s"(lds), "s"(soff) : "memory");
}
DEV void dma1kp(v4u rs, int voff, int soff, int lds) {
  asm volatile("s_mov_b32 m0, %2\n\ts_nop 0\n\tbuffer_load_dwordx4 %0, %1, %3 offen lds" ::"v"(voff), "s"(rs), "s"(lds), "s"(soff) : "memory");
}
DEV void xpose(const u32* r, v4u& f0, v4u& f1) {
#pragma unroll
  for (int i = 0; i < 4; ++i) {
    f0[i] = __builtin_amdgcn_perm(r[2 * i + 1], r[2 * i], 0x05040100u);
    f1[i] = __builtin_amdgcn_perm(r[2 * i + 1], r[2 * i], 0x07060302u);
  }
}
template <int CTRL>
DEV float dppadd(float x) {
  return x + asf(__builtin_amdgcn_update_dpp(0, asu(x), CTRL, 0xF, 0xF, true));
}
DEV float rowsum(float x) {
  x = dppadd<0x128>(x);
  x = dppadd<0x124>(x);
  x = dppadd<0x122>(x);
  return dppadd<0x121>(x);
}
DEV float fold32(float a, float b) {
  auto r = __builtin_amdgcn_permlane32_swap(asu(a), asu(b), false, false);
  return asf(r[0]) + asf(r[1]);
}
DEV float fold16(float a, float b) {
  auto r = __builtin_amdgcn_permlane16_swap(asu(a), asu(b), false, false);
  return asf(r[0]) + asf(r[1]);
}
DEV float gsum(float x) { x = fold16(x, x); return fold32(x, x); }
DEV float gmax(float x) {
  auto r = __builtin_amdgcn_permlane16_swap(asu(x), asu(x), false, false);
  x = fmaxf(asf(r[0]), asf(r[1]));
  auto q = __builtin_amdgcn_permlane32_swap(asu(x), asu(x), false, false);
  return fmaxf(asf(q[0]), asf(q[1]));
}
DEV u32 umax(u32 a, u32 b) { return a > b ? a : b; }
DEV u32 max4(v4u x, u32 m) {
  m = umax(umax(x[0], x[1]), m);
  return umax(umax(x[2], x[3]), m);
}
template <int N>
DEV void issue(v4u (&f)[N], rsrc_t rs, int voff) {
#pragma unroll
  for (int i = 0; i < N; ++i) f[i] = ld128c(rs, voff + (i & 3) * 1024, (i >> 2) * 4096);
}
template <int N>
DEV bool pending(const v4u (&f)[N]) {
  u32 m = 0;
#pragma unroll
  for (int i = 0; i < N; ++i) m = max4(f[i], m);
  return __builtin_amdgcn_ballot_w64(m == SENT) != 0ull;
}
DEV bool pend1(v4u f) { return __builtin_amdgcn_ballot_w64(max4(f, 0u) == SENT) != 0ull; }
template <int N>
DEV void settle(v4u (&f)[N], rsrc_t rs, int voff, int& budget) {
  if (pending<N>(f)) {
    bool again;
    do {
      __builtin_amdgcn_s_sleep(PSLEEP);
      asm volatile("" ::: "memory");
#pragma unroll
      for (int i = 0; i < N; ++i)
        if (pend1(f[i])) f[i] = ld128c(rs, voff + (i & 3) * 1024, (i >> 2) * 4096);
      --budget;
      again = pending<N>(f) & (budget > 0);
    } while (again);
  }
}
DEV float vfma(float a, float b, float c) { return __builtin_fmaf(a, b, c); }
DEV float vfma_s(float a, float sb, float c) { return __builtin_fmaf(a, sb, c); }
DEV float vmul(float a, float b) { return a * b; }
DEV bool all4(v4u f, u32 tag) { return __builtin_amdgcn_ballot_w64(!((f[0] == tag) & (f[1] == tag) & (f[2] == tag) & (f[3] == tag))) == 0ull; }
DEV void wait_flags(v4u f, rsrc_t rs, int voff, u32 tag, int& budget) {
  while (!all4(f, tag) & (budget > 0)) {
    __builtin_amdgcn_s_sleep(1);
    asm volatile("" ::: "memory");
    f = ld128c(rs, voff, 0);
    --budget;
  }
  asm volatile("" ::: "memory");
}
DEV float silu(float x) { return x * __builtin_amdgcn_rcpf(1.f + __builtin_amdgcn_exp2f(-1.44269504f * x)); }

#define STAMP(k) do { } while (0)

__global__ void __launch_bounds__(512, 2) NOPK
fused(const void* __restrict__ px, const void* __restrict__ pwqkv, const void* __restrict__ pwo, const void* __restrict__ pw1,
      const void* __restrict__ pw2, const void* __restrict__ pc, const void* __restrict__ pwm, void* pout, void* pws) {
  extern __shared__ __attribute__((aligned(16))) char smem[];
  LAS char* L = (LAS char*)smem;
  STAMP(0);
  const int tid = threadIdx.x, lane = tid & 63, w = __builtin_amdgcn_readfirstlane(tid >> 6);
  const int c = lane & 15, g = lane >> 4, l16 = lane * 16;
  const int b = blockIdx.x, xcd = b & 7, j = b >> 3, cl = xcd * 4 + (j >> 3), rank = j & 7, t0 = cl * 64;
  int budget = SPIN_CAP;

  u32 codepf;
  {
    u64 pcv;
    u32 clen;
    asm volatile("s_getpc_b64 %0\n.Lfk_pc0:\n\ts_mov_b32 %1, .Lfk_pc1-.Lfk_pc0" : "=s"(pcv), "=s"(clen));
    codepf = ld32(mkrs((const void*)pcv, w == 0 ? (int)clen : 0), lane * 128 + (j & (CODE_SLICES - 1)) * 8192, 0);
  }
  asm volatile("" ::"s"(px), "s"(pwqkv), "s"(pwo), "s"(pw1), "s"(pw2), "s"(pc), "s"(pwm), "s"(pout), "s"(pws));
  const rsrc_t rws = mkrs(pws, WS_END);
  const u32 tag = *(const __attribute__((address_space(4))) u32*)((const char*)pws + WS_EPOCH) + 1u;
  const int rr = (rank + xcd) & 7, pk_ = 4 * rr + (j >> 3);
  const bool packer = rr < 7;
  const int uq = xcd + 8 * (j >> 4), ukh = (j >> 3) & 1;
  const bool prodL = rank == 7 && packer;
  u32 rm[32];
  u32 cv;
  const rsrc_t rc = mkrs(pc, D * 2), rwm = mkrs(pwm, D * 6 * D * 2);
  auto arrive = [&]() LAM {
    asm volatile("s_waitcnt lgkmcnt(0)" ::: "memory");
    u32 old = 0;
    if (lane == 0) old = __hip_atomic_fetch_add((LAS u32*)(L + L_CNT), 1u, __ATOMIC_RELAXED, __HIP_MEMORY_SCOPE_WORKGROUP);
    const bool last = __builtin_amdgcn_readfirstlane(old) == 6;
    asm volatile("" ::: "memory");
    return last;
  };
  auto silu_put = [&](u32 cv) LAM {
    const float v = silu(lo16(cv));
    *(LAS unsigned short*)(L + L_SC + w * 128 + lane * 2) = (unsigned short)pk(v, v);
  };
  const f32x4 z4 = {0.f, 0.f, 0.f, 0.f};
  const rsrc_t rx = mkrs(px, S * D * 2);
  v4u xr[10];
  int xa[5];
  xa[0] = rank >= 5 ? rank - 5 : rank;
#pragma unroll
  for (int a = 1; a < 5; ++a) xa[a] = xa[a - 1] == 4 ? 0 : xa[a - 1] + 1;
  auto x_load = [&]() LAM {
#pragma unroll
    for (int i = 0; i < 10; ++i) {
      int tok = t0 - 16 + w + 16 * xa[i >> 1] + 8 * (i & 1);
      tok = tok < 0 ? 0 : tok;
      xr[i] = ld128(rx, lane < 56 ? l16 : OOB, tok * 896);
    }
  };
  u32 ra[8], rb[8];
  const int ks = pk_ >> 1;
  if (packer) {
    if (w < 3) {
      const int pr = 3 * (pk_ & 1) + w;
      const rsrc_t rs = mkrs(pwqkv, D * 3 * D * 2);
      const int vo = c < 14 ? g * 8 * 2688 + c * 4 : OOB;
      const int so = ks * 32 * 2688 + (pr >> 1) * 896 + (pr & 1) * 56 + rank * 112;
#pragma unroll
      for (int jj = 0; jj < 8; ++jj) ra[jj] = ld32(rs, vo + (jj & 1) * 2688, so + (jj >> 1) * 5376);
    } else if (w < 7) {
      const int jw = 4 * (pk_ & 1) + w - 3;
      const rsrc_t rs = mkrs(pw1, D * F * 2);
      const int v1 = jw < 7 ? g * 4 * 3584 + c * 4 : OOB, s1 = ks * 32 * 3584 + rank * 448 + jw * 64;
#pragma unroll
      for (int jj = 0; jj < 8; ++jj) ra[jj] = ld32(rs, v1 + (jj & 1) * 3584, s1 + ((jj & 2) + (jj >> 2) * 16) * 3584);
    }
    if (prodL && tid == 0) *(LAS u32*)(L + L_CNT) = 0u;
    SB();
    x_load();
    SB();
    if (w < 7) {
      v4u f0, f1;
      xpose(ra, f0, f1);
      const int jw = 4 * (pk_ & 1) + w - 3;
      const int dst = w < 3 ? WS_PQ + ((rank * 12 + (3 * (pk_ & 1) + w) * 2) * 14 + ks) * 1024 : WS_P1 + rank * 200704 + (jw * 14 + ks) * 2048;
      const int vo = jw < 7 ? l16 : OOB;
      st128c(f0, rws, vo, dst);
      st128c(f1, rws, vo, dst + (w < 3 ? 14336 : 1024));
      if (w < 3) drain();
    } else {
      const int q = rank * 28 + pk_;
      ra[0] = ld32(mkrs(pwo, D * D * 2), lane < 14 ? q * 1792 + lane * 128 : OOB, 0);
      ra[1] = ld32(rwm, lane < 56 ? (2 * q + (lane >= 28)) * 5376 + 1792 + (lane >= 28 ? lane - 28 : lane) * 128 : OOB, 0);
    }
  } else {
    const int vo = (w < 7 && uq < 14) ? (ukh * 224 + w * 32 + g * 8) * 5376 + uq * 128 + c * 4 : OOB;
    cv = ld16(rc, ukh * 448 + w * 64 + lane * 2, 0);
#pragma unroll
    for (int i = 0; i < 16; ++i) rm[i] = ld32(rwm, vo + (i >> 3) * 64, (i & 7) * 5376);
    if (tid == 0) *(LAS u32*)(L + L_CNT) = 0u;
    bar();
    if (w < 7 && uq < 14) {
      silu_put(cv);
      const v4u sf = lds128(L, L_SC + w * 128 + g * 16);
#pragma unroll
      for (int hh = 0; hh < 2; ++hh) {
        v4u f0, f1;
        xpose(rm + 8 * hh, f0, f1);
        const f32x4 a0 = mfma(f0, sf, z4), a1 = mfma(f1, sf, z4);
        if (c == 0) {
          stsf4(L, L_MODP + w * 256 + hh * 128 + g * 32, a0);
          stsf4(L, L_MODP + w * 256 + hh * 128 + g * 32 + 16, a1);
        }
      }
      if (arrive()) {
        float s0 = 0.f;
#pragma unroll
        for (int k = 0; k < 7; ++k) s0 += *(LAS float*)(L + L_MODP + k * 256 + (lane >> 3) * 32 + (lane & 1) * 16 + (lane & 6) * 2);
        st64c(v2u{asu(s0), tag}, rws, WS_MU + (uq * 64 + lane) * 16 + ukh * 8, 0);
      }
    }
    x_load();
  }
  STAMP(1);
  const int o_cl = WS_O + cl * 65536, x_cl = WS_X + cl * 65536, h_cl = WS_H + cl * H_CL;
#pragma unroll
  for (int i = 0; i < 10; ++i) pin(xr[i]);
  const int fa_own = lane == 0 ? WS_FA + rank * 1024 + pk_ * 32 + w * 4 : OOB;
  if (w < 3) st32c(tag, rws, fa_own, 0);
  STAMP(2);
  if (w < 4 && packer) {
    const rsrc_t rw2 = mkrs(pw2, F * D * 2);
    const int vo = rank < 7 ? g * 8 * 896 + rank * 128 + (c >> 2) * 8 + (c & 1) * 4 + (c & 2) * 16 : OOB;
    const int so = (pk_ * 64 + ((w >> 1) & 1) * 32) * 896 + (w & 1) * 64;
#pragma unroll
    for (int jj = 0; jj < 8; ++jj) rb[jj] = ld32(rw2, vo + (jj & 3) * 896, so + (jj >> 2) * 3584);
  }
  if (prodL) {
    const int vo = (w * 64 + g * 8) * 5376 + 1792 + pk_ * 128 + c * 4;
    cv = ld16(rc, w * 128 + lane * 2, 0);
#pragma unroll
    for (int i = 0; i < 32; ++i) rm[i] = ld32(rwm, vo + ((i >> 3) & 1) * 64, ((i >> 4) * 32 + (i & 7)) * 5376);
  }
  f32x2 t[10][4];
  v4u m[4];
  const int mo = w >= 4 ? WS_MU + (w - 4) * 3584 + (lane < 56 ? lane : 55) * 64 : OOB;
  {
    float sm[12], sq[12];
#pragma unroll
    for (int i = 0; i < 10; ++i) sm[i] = sq[i] = 0.f;
#pragma unroll
    for (int e = 0; e < 4; ++e)
#pragma unroll
      for (int i = 0; i < 10; ++i) {
        sm[i] = dot2(xr[i][e], 0x3F803F80u, sm[i]);
        sq[i] = dot2(xr[i][e], xr[i][e], sq[i]);
      }
    sm[10] = sm[11] = sq[10] = sq[11] = 0.f;
    float rs_[3], nn[3];
#pragma unroll
    for (int q = 0; q < 3; ++q) {
      const float m = rowsum(fold16(fold32(sm[4 * q], sm[4 * q + 2]), fold32(sm[4 * q + 1], sm[4 * q + 3]))) * (1.f / D);
      const float v = rowsum(fold16(fold32(sq[4 * q], sq[4 * q + 2]), fold32(sq[4 * q + 1], sq[4 * q + 3]))) * (1.f / D);
      rs_[q] = __builtin_amdgcn_rsqf(fmaxf(v - m * m, 0.f) + 1e-5f);
      nn[q] = -m * rs_[q];
      if (c == 0 && 4 * q + g < 10) *(LAS float*)(L + L_MU1 + (w + 16 * (g < 2 ? xa[2 * q] : xa[q < 2 ? 2 * q + 1 : 0]) + 8 * (g & 1)) * 4) = m;
    }
    SB();
    if (w == 7 && packer) asm volatile("" ::"v"(ra[0]), "v"(ra[1]));
#pragma unroll
    for (int i = 0; i < 4; ++i) m[i] = ld128c(rws, mo + i * 16, 0);
  if (rank < 7) {
    const u64 wa = (u64)pwo;
    const v4u rso = {(u32)wa, (u32)(wa >> 32) & 0xFFFFu, (u32)(D * D * 2), 0x00020000u};
    const int r8 = lane >> 3, ch = lane & 7;
    const int v0 = r8 * 896 + rank * 128 + ((ch ^ (r8 & 2)) << 4), v1 = v0 ^ 64;
#pragma unroll
    for (int i = 0; i < 7; ++i) dma1kp(rso, ((w + i) & 1) ? v1 : v0, (w * 7 + i) * 7168, L_B + (w * 7 + i) * 1024);
    if (tid < 32) sts128(L, L_B + 57344 + tid * 16, v4u{0u, 0u, 0u, 0u});
  }
  SB();
#pragma unroll
    for (int i = 0; i < 10; ++i) {
      const float r = asf(__builtin_amdgcn_readlane(asu(rs_[i >> 2]), 16 * (i & 3)));
      float nv = asf(__builtin_amdgcn_readlane(asu(nn[i >> 2]), 16 * (i & 3)));
      asm("" : "+v"(nv));
#pragma unroll
      for (int e = 0; e < 4; ++e) {
        t[i][e] = fma2(up2(xr[i][e]), f32x2{r, r}, f32x2{nv, nv});
        asm volatile("" : "+v"(t[i][e]));
      }
    }
  }
  asm volatile("" ::"v"(codepf));
  STAMP(3);
  if (w >= 3 && w < 7) {
    waitvm<4>();
    st32c(tag, rws, fa_own, 0);
  }
  if (w < 4) {
    if (packer) {
      v4u f0, f1;
      xpose(rb, f0, f1);
      const int d2 = WS_P2 + rank * 229376 + (pk_ * 4 + w) * 2048;
      st128c(f0, rws, rank < 7 ? l16 : OOB, d2);
      st128c(f1, rws, rank < 7 ? l16 + 1024 : OOB, d2);
    }
    const v4u sv = {SENT, SENT, SENT, SENT};
    st128c(sv, rws, l16, o_cl + (w * 16 + 2 * rank) * 1024);
    st128c(sv, rws, l16 + 1024, o_cl + (w * 16 + 2 * rank) * 1024);
    if (rank < 7) {
      st128c(sv, rws, l16, x_cl + (w * 14 + 2 * rank) * 1024);
      st128c(sv, rws, l16 + 1024, x_cl + (w * 14 + 2 * rank) * 1024);
      st128c(sv, rws, g == 0 ? l16 : OOB, x_cl + XS_OFF + (w * 7 + rank) * 256);
    }
  } else {
    for (;;) {
      bool ok = true;
#pragma unroll
      for (int i = 0; i < 4; ++i) ok = ok & (m[i][1] == tag) & (m[i][3] == tag);
      --budget;
      if ((__builtin_amdgcn_ballot_w64(!ok) == 0ull) | (budget <= 0)) break;
      asm volatile("" ::: "memory");
#pragma unroll
      for (int i = 0; i < 4; ++i) m[i] = ld128c(rws, mo + i * 16, 0);
    }
    u32 p0 = pk(asf(m[0][0]) + asf(m[0][2]), asf(m[1][0]) + asf(m[1][2])), p1 = pk(asf(m[2][0]) + asf(m[2][2]), asf(m[3][0]) + asf(m[3][2]));
    if (w >= 6) {
      p0 = pk(1.f + lo16(p0), 1.f + hi16(p0));
      p1 = pk(1.f + lo16(p1), 1.f + hi16(p1));
    }
    if (lane < 56) stsf4(L, L_PAIR + (w - 4) * 896 + l16, f32x4{lo16(p0), hi16(p0), lo16(p1), hi16(p1)});
  }
  bar();
  STAMP(4);
  const int fao = w == 7 ? WS_FA + rank * 1024 + (lane & 31) * 32 + (lane < 32 ? 0 : 4) : OOB;
  v2u fa = ld64c(rws, fao, 0);
  SB();
  if (lane < 56) {
    const f32x4 ca[2] = {ldsf4(L, L_PAIR + 1792 + 2 * l16), ldsf4(L, L_PAIR + 1808 + 2 * l16)}, sa[2] = {ldsf4(L, L_PAIR + 2 * l16), ldsf4(L, L_PAIR + 16 + 2 * l16)};
    const int base = L_A + ((lane >> 2) * 64 + (lane & 3) * 16 + (w ^ (lane & 7))) * 16;
#pragma unroll
    for (int i = 0; i < 10; ++i) {
      v4u hv;
      hv[0] = pk2(fma2(t[i][0], ca[0].xy, sa[0].xy));
      hv[1] = pk2(fma2(t[i][1], ca[0].zw, sa[0].zw));
      hv[2] = pk2(fma2(t[i][2], ca[1].xy, sa[1].xy));
      hv[3] = pk2(fma2(t[i][3], ca[1].zw, sa[1].zw));
      sts128(L, base + xa[i >> 1] * 14336 + (i & 1) * 128, hv);
    }
  }
  if (prodL && w < 7) {
    silu_put(cv);
    const v4u sf0 = lds128(L, L_SC + w * 128 + g * 16), sf1 = lds128(L, L_SC + w * 128 + 64 + g * 16);
#pragma unroll
    for (int hh = 0; hh < 2; ++hh) {
      v4u f0, f1;
      xpose(rm + 8 * hh, f0, f1);
      f32x4 a0 = mfma(f0, sf0, z4), a1 = mfma(f1, sf0, z4);
      xpose(rm + 16 + 8 * hh, f0, f1);
      a0 = mfma(f0, sf1, a0);
      a1 = mfma(f1, sf1, a1);
      if (c == 0) {
        stsf4(L, L_MODP + w * 256 + hh * 128 + g * 32, a0);
        stsf4(L, L_MODP + w * 256 + hh * 128 + g * 32 + 16, a1);
      }
    }
    if (arrive()) {
      float s0 = 0.f, s1 = 0.f;
#pragma unroll
      for (int k = 0; k < 7; ++k) {
        s0 += *(LAS float*)(L + L_MODP + k * 256 + ((lane >> 2) & 7) * 32 + (lane & 3) * 4);
        s1 += *(LAS float*)(L + L_MODP + k * 256 + ((lane >> 2) & 7) * 32 + (lane & 3) * 4 + 16);
      }
      st64c(v2u{pkn(s0, s1), tag}, rws, lane < 32 ? WS_MOD + (pk_ * 32 + lane) * 8 : OOB, 0);
    }
  }
  if (w == 7) {
    while ((__builtin_amdgcn_ballot_w64((fa[0] != tag) | (fa[1] != tag)) != 0ull) & (budget > 0)) {
      asm volatile("" ::: "memory");
      fa = ld64c(rws, fao, 0);
      --budget;
    }
  }
  bar();
  STAMP(5);
  const int kh = w >> 2;
  v4u wqf[21];
  const int pe = WS_PQ + ((rank * 12 + 4 + w) * 14 + 7 * kh) * 1024, po = WS_PQ + ((rank * 12 + 4 + w) * 14 + 7 - 7 * kh) * 1024;
  const int pq = WS_PQ + ((rank * 12 + (w & 3)) * 14 + 7 * kh) * 1024;
  auto wq_load = [&](int n) LAM {
    const int i = n / 3, k = n - 3 * i;
    wqf[n] = ld128c(rws, l16 + (i & 3) * 1024, (k == 0 ? pe : k == 1 ? pq : po) + (i >> 2) * 4096);
  };
#pragma unroll
  for (int n = 0; n < NQW; ++n) wq_load(n);
  SB();

  v4u w1f[28];
  const int w1o = WS_P1 + rank * 200704 + w * 28672, w1v = w < 7 ? l16 : OOB;
  auto w1_load = [&](int n) LAM { w1f[n] = ld128c(rws, w1v + (n & 3) * 1024, w1o + (n >> 2) * 4096); };
  f32x4 akv[5], aq[4];
  const int fbo = w == 0 ? WS_FB + (lane < 32 ? rank * 512 + lane * 16 : (lane & 7) * 512 + cl * 16) : OOB;
  {
    const int hb0 = L_A + (g * 16 + (c ^ g)) * 16, hb1 = L_A + (g * 16 + (c ^ (4 + g))) * 16;
    const int ad[2][2] = {{(kh ? hb1 : hb0) + kh * 7168, (kh ? hb0 : hb1) + kh * 7168}, {(kh ? hb0 : hb1) + (1 - kh) * 7168, (kh ? hb1 : hb0) + (1 - kh) * 7168}};
#pragma unroll
    for (int r = 0; r < 5; ++r) akv[r] = f32x4{0.f, 0.f, 0.f, 0.f};
#pragma unroll
    for (int r = 0; r < 4; ++r) aq[r] = f32x4{0.f, 0.f, 0.f, 0.f};
    v4u hf[5], mm[7];
    u32 f1;
    const int f1o = lane < 14 ? WS_FA + rank * 1024 + (2 * lane + (w >> 2)) * 32 + (3 + (w & 3)) * 4 : OOB;
    const u64 f1m = w < 7 ? 0x3FFFull : 0ull;
    const int mmo = w == 1 ? l16 : OOB;
#pragma unroll
    for (int r = 0; r < 5; ++r) hf[r] = lds128(L, ad[0][0] + r * 14336);
    SB();
#pragma unroll
    for (int s = 0; s < 14; ++s) {
#pragma unroll
      for (int r = 0; r < 5; ++r) {
        akv[r] = mfma(wqf[3 * (s >> 1) + 2 * (s & 1)], hf[r], akv[r]);
        if (!(s & 1) && r > 0) aq[r - 1] = mfma(wqf[3 * (s >> 1) + 1], hf[r], aq[r - 1]);
        if (s < 13) hf[r] = lds128(L, ad[(s + 1) & 1][((s + 1) >> 1) & 1] + r * 14336 + ((s + 1) >> 1) * 1024);
        if (r == 0 && NQW + 3 * (s >> 1) + 2 * (s & 1) < 21) wq_load(NQW + 3 * (s >> 1) + 2 * (s & 1));
        if (r == 3 && !(s & 1) && NQW + 3 * (s >> 1) + 1 < 21) wq_load(NQW + 3 * (s >> 1) + 1);
        if (s == 3 && r == 4 && w < 4) {
          drain();
          st32c(tag, rws, lane == 0 ? WS_FB + rank * 512 + cl * 16 + w * 4 : OOB, 0);
        }
        if (s == 8 && r == 2) f1 = ld32c(rws, f1o, 0);
        if (s == 11 && r == 4) {
          while (((__builtin_amdgcn_ballot_w64(f1 != tag) & f1m) != 0ull) & (budget > 0)) {
            asm volatile("" ::: "memory");
            f1 = ld32c(rws, f1o, 0);
            --budget;
          }
          asm volatile("" ::: "memory");
        }
        if (s >= 12 && (r == 0 || r == 3)) w1_load(2 * (s - 12) + (r ? 1 : 0));
        if (s == 10 && r == 0) {
#pragma unroll
          for (int i = 0; i < 7; ++i) mm[i] = ld128c(rws, mmo + (i & 3) * 1024, WS_MOD + (i >> 2) * 4096);
        }
        SB();
      }
    }
    if (w == 1) {
      for (;;) {
        bool ok = true;
#pragma unroll
        for (int i = 0; i < 7; ++i) ok = ok & (mm[i][1] == tag) & (mm[i][3] == tag);
        --budget;
        if ((__builtin_amdgcn_ballot_w64(!ok) == 0ull) | (budget <= 0)) break;
        asm volatile("" ::: "memory");
#pragma unroll
        for (int i = 0; i < 7; ++i) mm[i] = ld128c(rws, mmo + (i & 3) * 1024, WS_MOD + (i >> 2) * 4096);
      }
      const int la = L_GA + (lane & ~7) * 8 + (lane & 3) * 16 + (lane & 4) * 2;
#pragma unroll
      for (int i = 0; i < 7; ++i) {
        const int q = i * 64 + lane;
        const float one = (q >= 224 && q < 336) ? 1.f : 0.f;
        const v2u o = {pk(one + lo16(mm[i][0]), one + hi16(mm[i][0])), pk(one + lo16(mm[i][2]), one + hi16(mm[i][2]))};
        sts64(L, la + i * 512, o);
      }
    }
  }
  const u64 wsa = (u64)pws;
  const v4u rsw = {(u32)wsa, (u32)(wsa >> 32) & 0xFFFFu, rank < 7 ? (u32)WS_END : 0u, 0x00020000u};
  STAMP(6);
  bar();
  const v4u fb = ld128c(rws, fbo, 0);
#pragma unroll
  for (int n = 4; n < 8; ++n) w1_load(n);
  SB();
  {
    const int qb = L_A + ((w >> 1) & 1) * QK_HD + l16;
#pragma unroll
    for (int r = 0; r < 5; ++r) sts64(L, qb + (kh * 5 + r) * 1024 + (w & 1) * 8, v2u{pk(akv[r][0], akv[r][1]), pk(akv[r][2], akv[r][3])});
#pragma unroll
    for (int r = 0; r < 4; ++r) stsf4(L, qb + (10 + kh * 8 + 2 * r + (w & 1)) * 1024, aq[r]);
  }
  bar();
  {
    const int rt = w >> 1, hd = w & 1;
    const int qb = L_A + hd * QK_HD + l16 + rt * 1024;
    const int qq = L_A + hd * QK_HD + l16 + (10 + 2 * rt) * 1024;
    const f32x4 qa = ldsf4(L, qq) + ldsf4(L, qq + 8192), qc = ldsf4(L, qq + 1024) + ldsf4(L, qq + 9216);
    const v4u k0 = lds128(L, qb), k1 = lds128(L, qb + 1024);
    const int vb = L_A + hd * QK_HD + (5 + rt) * 1024 + ((c & 3) * 16 + 4 * g + (c >> 2)) * 16;
    const v2u v00 = trread(L, vb), v01 = trread(L, vb + 8), v10 = trread(L, vb + 1024), v11 = trread(L, vb + 1032);
    const v4u qf = {pk(qa[0], qa[1]), pk(qa[2], qa[3]), pk(qc[0], qc[1]), pk(qc[2], qc[3])};
    const f32x4 z = {0.f, 0.f, 0.f, 0.f};
    const f32x4 s0 = mfma(k0, qf, z), s1 = mfma(k1, qf, z);
    const int tq = t0 + 16 * rt + c;
    float sv[8];
    float mx = -3.0e38f;
#pragma unroll
    for (int e = 0; e < 8; ++e) {
      const int delta = c + (e < 4 ? 16 : 0) - 4 * g - (e & 3);
      const bool ok = (delta & ~14) == 0 && delta <= tq;
      sv[e] = ok ? (e < 4 ? s0[e & 3] : s1[e & 3]) * (0.18898223650461362f * 1.44269504088896341f) : -3.0e38f;
      mx = fmaxf(mx, sv[e]);
    }
    SB();
#pragma unroll
    for (int n = 8; n < 12; ++n) w1_load(n);
    SB();
    mx = gmax(mx);
    float sum = 0.f;
#pragma unroll
    for (int e = 0; e < 8; ++e) {
      sv[e] = __builtin_amdgcn_exp2f(sv[e] - mx);
      sum += sv[e];
    }
    const float inv = __builtin_amdgcn_rcpf(gsum(sum));
    v4u pf;
#pragma unroll
    for (int i = 0; i < 4; ++i) pf[i] = pk(sv[2 * i] * inv, sv[2 * i + 1] * inv);
    const f32x4 o0 = mfma(v4u{v00[0], v00[1], v10[0], v10[1]}, pf, z), o1 = mfma(v4u{v01[0], v01[1], v11[0], v11[1]}, pf, z);
    v4u of;
#pragma unroll
    for (int i = 0; i < 4; ++i) of[i] = canon(pk(o0[i], o1[i]));
    st128c(of, rws, l16, o_cl + (rt * 16 + 2 * rank + hd) * 1024);
  }
  STAMP(7);
  if (w == 0) wait_flags(fb, rws, fbo, tag, budget);
  SB();
  const int rt = w & 3;
  v4u x1p = {0u, 0u, 0u, 0u};
  if (rank < 7) {
    int wb[4];
#pragma unroll
    for (int x = 0; x < 4; ++x) wb[x] = L_B + (kh * 224 + 8 * g + (c >> 2)) * 128 + ((x ^ ((c >> 3) | ((g & 1) << 1))) << 5) + (c & 3) * 8;
    auto wo_read = [&](int q) LAM {
      const int hs = q >> 2, ct = q & 3;
      const v2u lo = trread(L, wb[ct ^ (((7 * hs) >> 1) & 1) * 2] + 28 * hs * 128);
      const v2u hi = trread(L, wb[ct ^ (((7 * hs + 1) >> 1) & 1) * 2] + (28 * hs + 4) * 128);
      return v4u{lo[0], lo[1], hi[0], hi[1]};
    };
    v4u wo[16];
#pragma unroll
    for (int q = 0; q < 16; ++q) wo[q] = wo_read(q);
    const int vo = rank * 128 + kh * 64 + g * 16;
    const v4u ga = lds128(L, L_GA + vo), cb = lds128(L, L_CB + vo);
    const float mu1 = *(LAS float*)(L + L_MU1 + (16 + 16 * rt + c) * 4);
    bar();
    v4u of[8];
    const int ovo = o_cl + (rt * 16 + 8 * kh) * 1024 + l16;
    issue<8>(of, rws, ovo);
    const int xo = c * 896 + g * 8 + (t0 + 16 * rt) * 896 + rank * 128 + kh * 64;
    const v2u xr0 = ld64(rx, xo, 0), xr1 = ld64(rx, xo + 32, 0);
    SB();
    settle<8>(of, rws, ovo, budget);
    STAMP(8);
    f32x4 acc[4];
#pragma unroll
    for (int q = 0; q < 4; ++q) acc[q] = f32x4{0.f, 0.f, 0.f, 0.f};
#pragma unroll
    for (int q = 0; q < 32; ++q) {
      acc[q & 3] = mfma(wo[q & 15], of[q >> 2], acc[q & 3]);
      if (q < 16) wo[q] = wo_read(16 + q);
      if (q % 2 == 1 && 12 + q / 2 < N1C) w1_load(12 + q / 2);
      SB();
    }
#pragma unroll
    for (int q = 0; q < 4; ++q) stsf4(L, L_A + (w * 4 + q) * 1024 + l16, acc[q]);
    bar();
    const int pb = L_A + (rt * 4 + 2 * kh) * 1024 + l16;
    const f32x4 a0 = ldsf4(L, pb) + ldsf4(L, pb + 16384), a1 = ldsf4(L, pb + 1024) + ldsf4(L, pb + 17408);
    const v4u xres = {xr0[0], xr0[1], xr1[0], xr1[1]};
    v4u xp;
    f32x2 s1v = {0.f, 0.f}, s2v = {0.f, 0.f};
#pragma unroll
    for (int i = 0; i < 4; ++i) {
      const u32 ao = pk2(i == 0 ? a0.xy : i == 1 ? a0.zw : i == 2 ? a1.xy : a1.zw);
      const u32 y = pk2(up2(ga[i]) * up2(ao));
      x1p[i] = pk2(up2(xres[i]) + up2(y));
      const f32x2 d = up2(x1p[i]) - f32x2{mu1, mu1};
      s1v += d;
      s2v = fma2(d, d, s2v);
      xp[i] = canon(pk2(d * up2(cb[i])));
    }
    float s1 = s1v.x + s1v.y, s2 = s2v.x + s2v.y;
    st128c(xp, rws, l16, x_cl + (rt * 14 + 2 * rank + kh) * 1024);
    s1 = gsum(s1);
    s2 = gsum(s2);
    st64c(v2u{canon(asu(s1)), canon(asu(s2))}, rws, g == 0 ? l16 : OOB, x_cl + XS_OFF + (rt * 7 + rank) * 256 + kh * 8);
  } else {
    bar();
#pragma unroll
    for (int n = 12; n < N1C; ++n) w1_load(n);
    bar();
  }
  STAMP(9);
  v4u w2f[28];
  f32x4 u0 = z4, u1 = z4;
  const int w2o = WS_P2 + rank * 229376 + w * 28672, w2v = rank < 7 ? l16 : OOB;
  auto w2_load = [&](int n) LAM { w2f[n] = ld128c(rws, w2v + (n & 3) * 1024, w2o + (n >> 2) * 4096); };
  constexpr int L_W2 = 57344, NST = 28 - N2B;
  static_assert(N2B == 16, "counted wait in the W1 GEMM");
  static_assert(L_W2 + 8 * NST * 1024 <= L_GA, "W2 staging");
  const v4u rsh = {(u32)wsa, (u32)(wsa >> 32) & 0xFFFFu, (u32)WS_END, 0x00020000u};
  {
    v4u xf[7], xs[1];
    const int pr = 4 * kh + g;
    const int xo = x_cl + (rt * 14 + 7 * kh) * 1024 + l16, so = pr < 7 ? x_cl + XS_OFF + (rt * 7 + pr) * 256 + c * 16 : OOB;
    const int vb = ((c & 1) ? L_SB : L_CB) + g * 16;
    {
      v4u bfv[N1C / 2];
#pragma unroll
      for (int s = 0; s < N1C / 2; ++s) bfv[s] = lds128(L, vb + s * 64);
      SB();
#pragma unroll
      for (int s = 0; s < N1C / 2; ++s) {
        u0 = mfma(w1f[2 * s], bfv[s], u0);
        u1 = mfma(w1f[2 * s + 1], bfv[s], u1);
      }
    }
    bar();
    issue<7>(xf, rws, xo);
    issue<1>(xs, rws, so);
    SB();
    settle<7>(xf, rws, xo, budget);
    settle<1>(xs, rws, so, budget);
#pragma unroll
    for (int n = N1C; n < 28; ++n) w1_load(n);
    SB();
#pragma unroll
    for (int i = 0; i < 7; ++i) sts128(L, L_A + (rt * 14 + 7 * kh + i) * 1024 + l16, xf[i]);
    const float s1 = gsum(asf(xs[0][0]) + asf(xs[0][2])), s2 = gsum(asf(xs[0][1]) + asf(xs[0][3]));
    if (g == 0) sts64(L, L_ST + ((rt * 2 + kh) * 16 + c) * 8, v2u{asu(s1), asu(s2)});
    if (tid == 0) *(LAS u32*)(L + L_CNT) = 0u;
    SB();
#pragma unroll
    for (int n = 0; n < 4; ++n) w2_load(n);
  }
  STAMP(10);
  bar();
  STAMP(11);
  const int fho = lane == 0 ? WS_FH + cl * 1024 + rank * 128 + w * 4 : OOB;
  const int fwo = (lane < 32 && rank < 7) ? WS_FH + cl * 1024 + w * 128 + lane * 4 : OOB;
  u32 fh;
  v4u hf[4][7];
  const int ho = rank < 7 ? h_cl + w * 28672 + l16 : OOB;
  const u64 live = rank < 7 ? ~0ull : 0ull;
  auto need = [&](u64 mask) LAM {
    while (((__builtin_amdgcn_ballot_w64(fh != tag) & mask & live) != 0ull) & (budget > 0)) {
      asm volatile("" ::: "memory");
      fh = ld32c(rws, fwo, 0);
      --budget;
    }
    asm volatile("" ::: "memory");
  };
  if (w < 7) {
    v4u xf[4];
#pragma unroll
    for (int i = 0; i < 4; ++i) xf[i] = lds128(L, L_A + i * 1024 + l16);
    const int ub = L_UV + w * 256 + g * 64;
    const int vb = ((c & 1) ? L_SB : L_CB) + g * 16;
    f32x4 uu[2], vv[2];
    SB();
    const float nl2e = -1.44269504f;
    auto flag = [&](int r) LAM {
      st32c(tag, rws, fho, r * 32);
      if (lane == 0) (void)__hip_atomic_fetch_add((LAS u32*)(L + L_CNT), 1u << (8 * r), __ATOMIC_RELAXED, __HIP_MEMORY_SCOPE_WORKGROUP);
    };
#pragma unroll
    for (int r = 0; r < 4; ++r) {
      f32x4 a[2] = {z4, z4};
      v2u sta, stb;
#pragma unroll
      for (int s = 0; s < 14; ++s) {
        const int n = r * 14 + s;
        a[0] = mfma(w1f[2 * s], xf[n & 3], a[0]);
        a[1] = mfma(w1f[2 * s + 1], xf[n & 3], a[1]);
        if (n + 4 < 56) xf[n & 3] = lds128(L, L_A + (n + 4) * 1024 + l16);
        if (s == 8) {
          sta = *(LAS v2u*)(L + L_ST + (r * 32 + c) * 8);
          stb = *(LAS v2u*)(L + L_ST + (r * 32 + 16 + c) * 8);
        }
        if (r == 0 && s >= N1C / 2) {
          const v4u bv = lds128(L, vb + s * 64);
          u0 = mfma(w1f[2 * s], bv, u0);
          u1 = mfma(w1f[2 * s + 1], bv, u1);
        }
        if (n >= 4 && n % 2 == 0 && 4 + (n - 4) / 2 < N2B) w2_load(4 + (n - 4) / 2);
        if (r > 0 && s == 11) {
          if (r == 1) waitvm<6>();
          else waitvm<0>();
          flag(r - 1);
        }
        if (r == 3 && s == 12) fh = ld32c(rws, fwo, 0);
        SB();
      }
      if (r == 0) {
        const float sc = (c & 1) ? nl2e : 1.f;
        if (c < 2) {
          stsf4(L, ub + c * 32, u0 * sc);
          stsf4(L, ub + c * 32 + 16, u1 * sc);
        }
        uu[0] = ldsf4(L, ub);
        uu[1] = ldsf4(L, ub + 16);
        vv[0] = ldsf4(L, ub + 32);
        vv[1] = ldsf4(L, ub + 48);
      }
      const float mu = (asf(sta[0]) + asf(stb[0])) * (1.f / D);
      const float var = fmaxf((asf(sta[1]) + asf(stb[1])) * (1.f / D) - mu * mu, 0.f);
      const float rsl = nl2e * __builtin_amdgcn_rsqf(var + 1e-5f);
      float y[8];
#pragma unroll
      for (int e = 0; e < 8; ++e) {
        const float z = vfma(vfma(-mu, uu[e & 1][e >> 1], a[e & 1][e >> 1]), rsl, vv[e & 1][e >> 1]);
        y[e] = z * __builtin_amdgcn_rcpf(vfma(__builtin_amdgcn_exp2f(z), nl2e, nl2e));
      }
      const v4u hv = {pkn(y[0], y[1]), pkn(y[2], y[3]), pkn(y[4], y[5]), pkn(y[6], y[7])};
      st128c(hv, rws, l16, h_cl + (rank * 28 + w + r * 7) * 1024);
      SB();
    }
    if (rank == 7) {
      drain();
      flag(3);
    }
  } else {
#pragma unroll
    for (int n = 4; n < N2B; ++n) w2_load(n);
    const int own = h_cl + rank * 28672 + l16;
    int warmed = 0;
#pragma unroll 1
    for (int ww = 0; ww < 8; ++ww) {
#pragma unroll
      for (int q = 0; q < NST; ++q) dma1k(rsw, l16, WS_P2 + rank * 229376 + (ww * 28 + N2B + q) * 1024, L_W2 + (ww * NST + q) * 1024);
      if (warmed < 3 && ((__builtin_amdgcn_readfirstlane(*(volatile LAS u32*)(L + L_CNT)) >> (8 * warmed)) & 255u) == 7u) {
#pragma unroll
        for (int i = 0; i < 7; ++i) dma1k(rsh, own + i * 1024, warmed * 7168, L_DUMMY);
        ++warmed;
      }
    }
    fh = ld32c(rws, fwo, 0);
    drain();
  }
  STAMP(12);
  if (rank < 7) {
    need(0x7F7Full);
    issue<7>(hf[0], rws, ho);
    issue<7>(hf[1], rws, ho + 7168);
    fh = ld32c(rws, fwo, 0);
    bar();
#pragma unroll
    for (int n = 0; n < NST; ++n) w2f[N2B + n] = lds128(L, L_W2 + (w * NST + n) * 1024 + l16);
    waitvm<15>();
    st32c(tag, rws, fho, 96);
#pragma unroll
    for (int r = 0; r < 4; ++r) {
      if (r == 1 || r == 2) {
        need(0x7Full << (8 + 8 * r));
        issue<7>(hf[r + 1], rws, ho + (r + 1) * 7168);
        if (r == 1) fh = ld32c(rws, fwo, 0);
      }
      SB();
      f32x4 acc[4];
#pragma unroll
      for (int q = 0; q < 4; ++q) acc[q] = z4;
#pragma unroll
      for (int s = 0; s < 7; ++s) {
#pragma unroll
        for (int q = 0; q < 4; ++q) acc[q] = mfma(w2f[4 * s + q], hf[r][s], acc[q]);
      }
      SB();
      if (r == 1) bar();
#pragma unroll
      for (int q = 0; q < 4; ++q) stsf4(L, ((r * 8 + w) * 4 + q) * 1024 + l16, acc[q]);
      SB();
    }
    STAMP(13);
    const v4u gb = lds128(L, L_GB + rank * 128 + kh * 64 + g * 16);
    bar();
    f32x4 pa[8][2];
#pragma unroll
    for (int p = 0; p < 8; ++p) {
      pa[p][0] = ldsf4(L, ((rt * 8 + p) * 4 + 2 * kh) * 1024 + l16);
      pa[p][1] = ldsf4(L, ((rt * 8 + p) * 4 + 2 * kh + 1) * 1024 + l16);
    }
    SB();
    f32x4 a0 = pa[0][0], a1 = pa[0][1];
#pragma unroll
    for (int p = 1; p < 8; ++p) {
      a0 += pa[p][0];
      a1 += pa[p][1];
    }
    v4u ov;
#pragma unroll
    for (int i = 0; i < 4; ++i) {
      const u32 mo = pk(a0[i], a1[i]);
      const u32 y = pk(lo16(gb[i]) * lo16(mo), hi16(gb[i]) * hi16(mo));
      ov[i] = pk(lo16(x1p[i]) + lo16(y), hi16(x1p[i]) + hi16(y));
    }
    const rsrc_t rout = mkrs(pout, S * D * 2);
    const int oo = c * 896 + g * 8 + (t0 + 16 * rt) * 896 + rank * 128 + kh * 64;
    __builtin_amdgcn_raw_buffer_store_b64(v2u{ov[0], ov[1]}, rout, oo, 0, 0);
    __builtin_amdgcn_raw_buffer_store_b64(v2u{ov[2], ov[3]}, rout, oo + 32, 0, 0);
  } else if (cl == 0 && w == 7) {
    for (;;) {
      asm volatile("" ::: "memory");
      bool ok = true;
#pragma unroll
      for (int i = 0; i < 4; ++i) {
        const v4u f = ld128c(rws, l16 + i * 1024, WS_FB);
        ok = ok & (f[0] == tag) & (f[1] == tag) & (f[2] == tag) & (f[3] == tag);
      }
      --budget;
      if ((__builtin_amdgcn_ballot_w64(!ok) == 0ull) | (budget <= 0)) break;
      __builtin_amdgcn_s_sleep(8);
    }
    if (lane == 0) st32c(tag, rws, WS_EPOCH, 0);
  }
  STAMP(14);
  asm volatile(".Lfk_pc1:");
}
}

extern "C" void kernel_launch(void* const* d_in, const int* in_sizes, int n_in, void* d_out, int out_size, void* d_ws,
                              size_t ws_size, hipStream_t stream) {
  (void)hipFuncSetAttribute((const void*)fk::fused, hipFuncAttributeMaxDynamicSharedMemorySize, fk::L_END);
  fk::fused<<<256, 512, fk::L_END, stream>>>(d_in[0], d_in[1], d_in[2], d_in[4], d_in[5], d_in[6], d_in[7], d_out, d_ws);
}
```

```cpp
#include <hip/hip_runtime.h>

typedef unsigned u32;
typedef unsigned long long u64;
typedef u32 v4u __attribute__((ext_vector_type(4)));
typedef u32 v2u __attribute__((ext_vector_type(2)));
typedef float f32x4 __attribute__((ext_vector_type(4)));
typedef float f32x2 __attribute__((ext_vector_type(2)));
typedef short bf16x8 __attribute__((ext_vector_type(8)));
typedef __bf16 bf16x2 __attribute__((ext_vector_type(2)));
typedef __amdgpu_buffer_rsrc_t rsrc_t;
#define LAS __attribute__((address_space(3)))
#define SB() __builtin_amdgcn_sched_barrier(0)
#define NOPK
#define DEV __device__ __forceinline__ NOPK
#define LAM __attribute__((always_inline)) NOPK
#define fmaxf __builtin_fmaxf

namespace fk {
constexpr int S = 2048, D = 448, F = 1792;
constexpr int WS_EPOCH = 0;
constexpr int WS_FA = 81920;
constexpr int WS_FB = 8192;
constexpr int WS_FH = 32768;
constexpr int WS_MOD = 16384;
constexpr int WS_MU = 65536;
constexpr int WS_O = 131072;
constexpr int WS_X = WS_O + 32 * 65536;
constexpr int XS_OFF = 57344;
constexpr int WS_H = WS_X + 32 * 65536;
constexpr int H_CL = 8 * 4 * 7 * 1024;
constexpr int WS_PQ = WS_H + 32 * H_CL;
constexpr int WS_PO = WS_PQ + 8 * 168 * 1024;
constexpr int WS_P2 = WS_PO + 8 * 65536;
constexpr int WS_P1 = WS_P2 + 8 * 224 * 1024;
constexpr int WS_END = WS_P1 + 8 * 196 * 1024;
constexpr int L_A = 0;
constexpr int L_B = 71680;
constexpr int L_PAIR = 137216;
constexpr int L_MODP = 141312;
constexpr int L_SC = 143360;
constexpr int L_MU1 = 154496;
constexpr int L_GA = 155648, L_SB = L_GA + 896, L_CB = L_SB + 896, L_GB = L_CB + 896;
constexpr int L_ST = L_GB + 896;
constexpr int L_UV = L_ST + 1024;
constexpr int L_CNT = L_UV + 1792;
constexpr int L_GO = L_CNT + 16, L_ARR = L_CNT + 32;
constexpr int L_DUMMY = L_CNT + 64;
constexpr int L_END = L_DUMMY + 1024;
static_assert(L_END <= 163840, "LDS");
constexpr int QK_HD = 26 * 1024;
constexpr u32 SENT = 0xFFFFFFFFu;
constexpr int SPIN_CAP = 1 << 16;
constexpr int OOB = 0x40000000;
constexpr int CODE_SLICES = 8;
#define PSLEEP 6
#define NPF 14
#define HFD 8
#define NQW 4
#define N1B 12
#define N1C 22
#define N2A 8
#define N2B 16

DEV float asf(u32 x) { return __builtin_bit_cast(float, x); }
DEV u32 asu(float x) { return __builtin_bit_cast(u32, x); }
DEV float lo16(u32 x) { return asf(x << 16); }
DEV float hi16(u32 x) { return asf(x & 0xFFFF0000u); }
DEV u32 pk(float a, float b) {
  f32x2 v = {a, b};
  return __builtin_bit_cast(u32, __builtin_convertvector(v, bf16x2));
}
DEV u32 pk2(f32x2 v) { return __builtin_bit_cast(u32, __builtin_convertvector(v, bf16x2)); }
DEV u32 pkn(float a, float b) {
  asm("" : "+v"(a));
  asm("" : "+v"(b));
  return pk(a, b);
}
DEV f32x2 up2(u32 x) { return f32x2{lo16(x), hi16(x)}; }
DEV f32x2 fma2(f32x2 a, f32x2 b, f32x2 c) { return __builtin_elementwise_fma(a, b, c); }
DEV u32 canon(u32 v) { return v < 0xFFFFFFFEu ? v : 0xFFFFFFFEu; }
DEV f32x4 mfma(v4u a, v4u b, f32x4 c) {
  return __builtin_amdgcn_mfma_f32_16x16x32_bf16(__builtin_bit_cast(bf16x8, a), __builtin_bit_cast(bf16x8, b), c, 0, 0, 0);
}
DEV float dot2(u32 a, u32 b, float c) {
  return __builtin_amdgcn_fdot2_f32_bf16(__builtin_bit_cast(bf16x2, a), __builtin_bit_cast(bf16x2, b), c, false);
}
DEV rsrc_t mkrs(const void* p, int bytes) { return __builtin_amdgcn_make_buffer_rsrc((void*)p, 0, bytes, 0x00020000); }
DEV u32 ld32(rsrc_t r, int v, int s) { return __builtin_amdgcn_raw_buffer_load_b32(r, v, s, 0); }
DEV v4u ld128(rsrc_t r, int v, int s) { return __builtin_amdgcn_raw_buffer_load_b128(r, v, s, 0); }
DEV u32 ld16(rsrc_t r, int v, int s) { return __builtin_amdgcn_raw_buffer_load_b16(r, v, s, 0); }
DEV v2u ld64(rsrc_t r, int v, int s) { return __builtin_amdgcn_raw_buffer_load_b64(r, v, s, 0); }
DEV v2u ld64c(rsrc_t r, int v, int s) { return __builtin_amdgcn_raw_buffer_load_b64(r, v, s, 16); }
DEV u32 ld32c(rsrc_t r, int v, int s) { return __builtin_amdgcn_raw_buffer_load_b32(r, v, s, 16); }
DEV v4u ld128c(rsrc_t r, int v, int s) { return __builtin_amdgcn_raw_buffer_load_b128(r, v, s, 16); }
DEV void st128c(v4u x, rsrc_t r, int v, int s) { __builtin_amdgcn_raw_buffer_store_b128(x, r, v + s, 0, 16); }
DEV void st64c(v2u x, rsrc_t r, int v, int s) { __builtin_amdgcn_raw_buffer_store_b64(x, r, v, s, 16); }
DEV void st32c(u32 x, rsrc_t r, int v, int s) { __builtin_amdgcn_raw_buffer_store_b32(x, r, v, s, 16); }
DEV v4u lds128(LAS char* L, int off) { return *(LAS v4u*)(L + off); }
DEV f32x4 ldsf4(LAS char* L, int off) { return *(LAS f32x4*)(L + off); }
DEV void sts128(LAS char* L, int off, v4u x) { *(LAS v4u*)(L + off) = x; }
DEV void stsf4(LAS char* L, int off, f32x4 x) { *(LAS f32x4*)(L + off) = x; }
DEV void sts64(LAS char* L, int off, v2u x) { *(LAS v2u*)(L + off) = x; }
typedef short v4s __attribute__((ext_vector_type(4)));
DEV v2u trread(LAS char* L, int off) { return __builtin_bit_cast(v2u, __builtin_amdgcn_ds_read_tr16_b64_v4i16((LAS v4s*)(L + off))); }
DEV void bar() {
  asm volatile("s_waitcnt lgkmcnt(0)" ::: "memory");
  __builtin_amdgcn_s_barrier();
  asm volatile("" ::: "memory");
}
DEV void drain() { asm volatile("s_waitcnt vmcnt(0)" ::: "memory"); }
template <int N>
DEV void waitvm() { asm volatile("s_waitcnt vmcnt(%0)" ::"n"(N) : "memory"); }
DEV void pin(v4u& x) { asm volatile("" : "+v"(x)); }
DEV void dma1k(v4u rs, int voff, int soff, int lds) {
  asm volatile("s_mov_b32 m0, %2\n\ts_nop 0\n\tbuffer_load_dwordx4 %0, %1, %3 offen sc1 lds" ::"v"(voff), "s"(rs), "s"(lds), "s"(soff) : "memory");
}
DEV void dma1kp(v4u rs, int voff, int soff, int lds) {
  asm volatile("s_mov_b32 m0, %2\n\ts_nop 0\n\tbuffer_load_dwordx4 %0, %1, %3 offen lds" ::"v"(voff), "s"(rs), "s"(lds), "s"(soff) : "memory");
}
DEV void dma4(v4u rs, int voff, int lds) {
  asm volatile("s_mov_b32 m0, %2\n\ts_nop 0\n\tbuffer_load_dword %0, %1, 0 offen lds" ::"v"(voff), "s"(rs), "s"(lds) : "memory");
}
DEV void xpose(const u32* r, v4u& f0, v4u& f1) {
#pragma unroll
  for (int i = 0; i < 4; ++i) {
    f0[i] = __builtin_amdgcn_perm(r[2 * i + 1], r[2 * i], 0x05040100u);
    f1[i] = __builtin_amdgcn_perm(r[2 * i + 1], r[2 * i], 0x07060302u);
  }
}
template <int CTRL>
DEV float dppadd(float x) {
  return x + asf(__builtin_amdgcn_update_dpp(0, asu(x), CTRL, 0xF, 0xF, true));
}
DEV float rowsum(float x) {
  x = dppadd<0x128>(x);
  x = dppadd<0x124>(x);
  x = dppadd<0x122>(x);
  return dppadd<0x121>(x);
}
DEV float fold32(float a, float b) {
  auto r = __builtin_amdgcn_permlane32_swap(asu(a), asu(b), false, false);
  return asf(r[0]) + asf(r[1]);
}
DEV float fold16(float a, float b) {
  auto r = __builtin_amdgcn_permlane16_swap(asu(a), asu(b), false, false);
  return asf(r[0]) + asf(r[1]);
}
DEV float gsum(float x) { x = fold16(x, x); return fold32(x, x); }
DEV float gmax(float x) {
  auto r = __builtin_amdgcn_permlane16_swap(asu(x), asu(x), false, false);
  x = fmaxf(asf(r[0]), asf(r[1]));
  auto q = __builtin_amdgcn_permlane32_swap(asu(x), asu(x), false, false);
  return fmaxf(asf(q[0]), asf(q[1]));
}
DEV u32 umax(u32 a, u32 b) { return a > b ? a : b; }
DEV u32 max4(v4u x, u32 m) {
  m = umax(umax(x[0], x[1]), m);
  return umax(umax(x[2], x[3]), m);
}
template <int N>
DEV void issue(v4u (&f)[N], rsrc_t rs, int voff) {
#pragma unroll
  for (int i = 0; i < N; ++i) f[i] = ld128c(rs, voff + (i & 3) * 1024, (i >> 2) * 4096);
}
template <int N>
DEV bool pending(const v4u (&f)[N]) {
  u32 m = 0;
#pragma unroll
  for (int i = 0; i < N; ++i) m = max4(f[i], m);
  return __builtin_amdgcn_ballot_w64(m == SENT) != 0ull;
}
DEV bool pend1(v4u f) { return __builtin_amdgcn_ballot_w64(max4(f, 0u) == SENT) != 0ull; }
template <int N>
DEV void settle(v4u (&f)[N], rsrc_t rs, int voff, int& budget) {
  if (pending<N>(f)) {
    bool again;
    do {
      __builtin_amdgcn_s_sleep(PSLEEP);
      asm volatile("" ::: "memory");
#pragma unroll
      for (int i = 0; i < N; ++i)
        if (pend1(f[i])) f[i] = ld128c(rs, voff + (i & 3) * 1024, (i >> 2) * 4096);
      --budget;
      again = pending<N>(f) & (budget > 0);
    } while (again);
  }
}
DEV float vfma(float a, float b, float c) { return __builtin_fmaf(a, b, c); }
DEV float vfma_s(float a, float sb, float c) { return __builtin_fmaf(a, sb, c); }
DEV float vmul(float a, float b) { return a * b; }
DEV bool all4(v4u f, u32 tag) { return __builtin_amdgcn_ballot_w64(!((f[0] == tag) & (f[1] == tag) & (f[2] == tag) & (f[3] == tag))) == 0ull; }
DEV void wait_flags(v4u f, rsrc_t rs, int voff, u32 tag, int& budget) {
  while (!all4(f, tag) & (budget > 0)) {
    __builtin_amdgcn_s_sleep(1);
    asm volatile("" ::: "memory");
    f = ld128c(rs, voff, 0);
    --budget;
  }
  asm volatile("" ::: "memory");
}
DEV float silu(float x) { return x * __builtin_amdgcn_rcpf(1.f + __builtin_amdgcn_exp2f(-1.44269504f * x)); }

#define STAMP(k) do { } while (0)

__global__ void __launch_bounds__(512, 2) NOPK
fused(const void* __restrict__ px, const void* __restrict__ pwqkv, const void* __restrict__ pwo, const void* __restrict__ pw1,
      const void* __restrict__ pw2, const void* __restrict__ pc, const void* __restrict__ pwm, void* pout, void* pws) {
  extern __shared__ __attribute__((aligned(16))) char smem[];
  LAS char* L = (LAS char*)smem;
  STAMP(0);
  const int tid = threadIdx.x, lane = tid & 63, w = __builtin_amdgcn_readfirstlane(tid >> 6);
  const int c = lane & 15, g = lane >> 4, l16 = lane * 16;
  const int b = blockIdx.x, xcd = b & 7, j = b >> 3, cl = xcd * 4 + (j >> 3), rank = j & 7, t0 = cl * 64;
  int budget = SPIN_CAP;

  u32 codepf;
  {
    u64 pcv;
    u32 clen;
    asm volatile("s_getpc_b64 %0\n.Lfk_pc0:\n\ts_mov_b32 %1, .Lfk_pc1-.Lfk_pc0" : "=s"(pcv), "=s"(clen));
    codepf = ld32(mkrs((const void*)pcv, w == 0 ? (int)clen : 0), lane * 128 + (j & (CODE_SLICES - 1)) * 8192, 0);
  }
  asm volatile("" ::"s"(px), "s"(pwqkv), "s"(pwo), "s"(pw1), "s"(pw2), "s"(pc), "s"(pwm), "s"(pout), "s"(pws));
  const rsrc_t rws = mkrs(pws, WS_END);
  const u32 tag = *(const __attribute__((address_space(4))) u32*)((const char*)pws + WS_EPOCH) + 1u;
  const int rr = (rank + xcd) & 7, pk_ = 4 * rr + (j >> 3);
  const bool packer = rr < 7;
  const f32x4 z4 = {0.f, 0.f, 0.f, 0.f};
  const rsrc_t rc = mkrs(pc, D * 2), rwm = mkrs(pwm, D * 6 * D * 2), rx = mkrs(px, S * D * 2);
  const int o_cl = WS_O + cl * 65536, x_cl = WS_X + cl * 65536, h_cl = WS_H + cl * H_CL;
  const int kh = w >> 2;
  v4u wqf[21];
  const int pe = WS_PQ + ((rank * 12 + 4 + w) * 14 + 7 * kh) * 1024, po = WS_PQ + ((rank * 12 + 4 + w) * 14 + 7 - 7 * kh) * 1024;
  const int pq = WS_PQ + ((rank * 12 + (w & 3)) * 14 + 7 * kh) * 1024;
  auto wq_load = [&](int n) LAM {
    const int i = n / 3, k = n - 3 * i;
    wqf[n] = ld128c(rws, l16 + (i & 3) * 1024, (k == 0 ? pe : k == 1 ? pq : po) + (i >> 2) * 4096);
  };
  auto slab2 = [&](int i) LAM {
    const u64 wa = rank < 7 ? (u64)pwo : (u64)pwm;
    const int st = rank < 7 ? 896 : 5376;
    const v4u rso = {(u32)wa, (u32)(wa >> 32) & 0xFFFFu, rank < 7 ? (u32)(D * D * 2) : packer ? (u32)(D * 6 * D * 2) : 0u, 0x00020000u};
    const int r8 = lane >> 3, ch = lane & 7;
    const int v0 = r8 * st + (rank < 7 ? rank * 128 : 1792 + pk_ * 128) + ((ch ^ (r8 & 2)) << 4), v1 = v0 ^ 64;
    dma1kp(rso, v0, i * 8 * st, L_B + i * 1024);
    dma1kp(rso, v1, (i + 1) * 8 * st, L_B + (i + 1) * 1024);
  };
  if (w == 0) {
    const int ks = pk_ >> 1;
    const int fa_own = lane == 0 ? WS_FA + rank * 1024 + pk_ * 32 : OOB;
    u32 rb[32];
    if (packer) {
      u32 ra[24];
      const rsrc_t rq = mkrs(pwqkv, D * 3 * D * 2), r1 = mkrs(pw1, D * F * 2);
      const int vo = c < 14 ? g * 8 * 2688 + c * 4 : OOB;
#pragma unroll
      for (int u = 0; u < 3; ++u) {
        const int pr = 3 * (pk_ & 1) + u, so = ks * 32 * 2688 + (pr >> 1) * 896 + (pr & 1) * 56 + rank * 112;
#pragma unroll
        for (int jj = 0; jj < 8; ++jj) ra[8 * u + jj] = ld32(rq, vo + (jj & 1) * 2688, so + (jj >> 1) * 5376);
      }
      SB();
#pragma unroll
      for (int u = 0; u < 3; ++u) {
        v4u f0, f1;
        xpose(ra + 8 * u, f0, f1);
        const int dst = WS_PQ + ((rank * 12 + (3 * (pk_ & 1) + u) * 2) * 14 + ks) * 1024;
        st128c(f0, rws, l16, dst);
        st128c(f1, rws, l16, dst + 14336);
      }
      SB();
      const int v1 = g * 4 * 3584 + c * 4, v1l = (pk_ & 1) ? OOB : v1, s1 = ks * 32 * 3584 + rank * 448 + (pk_ & 1) * 256;
#pragma unroll
      for (int i = 0; i < 32; ++i) rb[i] = ld32(r1, (i < 24 ? v1 : v1l) + (i & 1) * 3584 + (i >> 3) * 64, s1 + ((i & 2) + ((i >> 2) & 1) * 16) * 3584);
      waitvm<32>();
    }
    st32c(tag, rws, fa_own, 0);
    STAMP(1);
    u32 rd[32];
    if (packer) {
#pragma unroll
      for (int u = 0; u < 4; ++u) {
        v4u f0, f1;
        xpose(rb + 8 * u, f0, f1);
        const int dst = WS_P1 + rank * 200704 + ((4 * (pk_ & 1) + u) * 14 + ks) * 2048, vo1 = (u == 3 && (pk_ & 1)) ? OOB : l16;
        st128c(f0, rws, vo1, dst);
        st128c(f1, rws, vo1 + 1024, dst);
      }
      SB();
      const rsrc_t r2 = mkrs(pw2, F * D * 2);
      const int vo = g * 8 * 896 + rank * 128 + (c >> 2) * 8 + (c & 1) * 4 + (c & 2) * 16, so = pk_ * 64 * 896;
      if (rank < 7) {
#pragma unroll
        for (int i = 0; i < 32; ++i) rd[i] = ld32(r2, vo + (i & 3) * 896 + ((i >> 3) & 1) * 64, so + ((i >> 4) * 32 + (i & 4)) * 896);
      }
    }
    SB();
    u32 cv[7];
    if (rank == 7) {
#pragma unroll
      for (int i = 0; i < 7; ++i) cv[i] = ld16(rc, lane * 2 + i * 128, 0);
    }
    {
      v4u m[14];
      u32 fa;
      for (;;) {
        asm volatile("" ::: "memory");
#pragma unroll
        for (int i = 0; i < 14; ++i) m[i] = ld128c(rws, l16 + (i & 3) * 1024, WS_MU + (i >> 2) * 4096);
        fa = ld32c(rws, WS_FA + rank * 1024 + (lane & 31) * 32, 0);
        bool ok = fa == tag;
#pragma unroll
        for (int i = 0; i < 14; ++i) ok = ok & (m[i][1] == tag) & (m[i][3] == tag);
        --budget;
        if ((__builtin_amdgcn_ballot_w64(!ok) == 0ull) | (budget <= 0)) break;
        __builtin_amdgcn_s_sleep(PSLEEP);
      }
#pragma unroll
      for (int i = 0; i < 14; i += 2) {
        u32 p = pk(asf(m[i][0]) + asf(m[i][2]), asf(m[i + 1][0]) + asf(m[i + 1][2]));
        if (i >= 6) p = pk((i > 6 ? 1.f : 0.f) + lo16(p), 1.f + hi16(p));
        *(LAS float*)(L + L_PAIR + i * 256 + lane * 4) = lo16(p);
        *(LAS float*)(L + L_PAIR + (i + 1) * 256 + lane * 4) = hi16(p);
      }
      asm volatile("s_waitcnt lgkmcnt(0)" ::: "memory");
      *(volatile LAS u32*)(L + L_GO) = tag;
    }
    st32c(tag, rws, fa_own, 4);
    STAMP(2);
    if (packer) {
      SB();
      if (rank < 7) {
#pragma unroll
        for (int u = 0; u < 4; ++u) {
          v4u f0, f1;
          xpose(rd + 8 * u, f0, f1);
          const int dst = WS_P2 + rank * 229376 + (pk_ * 4 + u) * 2048;
          st128c(f0, rws, l16, dst);
          st128c(f1, rws, l16 + 1024, dst);
        }
      } else {
#pragma unroll
        for (int i = 0; i < 7; ++i) {
          const float v = silu(lo16(cv[i]));
          *(LAS unsigned short*)(L + L_SC + i * 128 + lane * 2) = (unsigned short)pk(v, v);
        }
      }
    }
    STAMP(3);
    asm volatile("" ::"v"(codepf));
#pragma unroll
    for (int n = 0; n < NQW; ++n) wq_load(n);
    STAMP(4);
  } else {
    const int v = w - 1;
    if (!packer) {
      const int uq = xcd + 8 * (j >> 4), ukh = (j >> 3) & 1;
      if (uq < 14) {
        u32 rm[16];
        const int vo = (ukh * 224 + v * 32 + g * 8) * 5376 + uq * 128 + c * 4;
        const u32 cv = ld16(rc, ukh * 448 + v * 64 + lane * 2, 0);
#pragma unroll
        for (int i = 0; i < 16; ++i) rm[i] = ld32(rwm, vo + (i >> 3) * 64, (i & 7) * 5376);
        SB();
        const float sv = silu(lo16(cv));
        *(LAS unsigned short*)(L + L_SC + v * 128 + lane * 2) = (unsigned short)pk(sv, sv);
        const v4u sf = lds128(L, L_SC + v * 128 + g * 16);
#pragma unroll
        for (int hh = 0; hh < 2; ++hh) {
          v4u f0, f1;
          xpose(rm + 8 * hh, f0, f1);
          const f32x4 a0 = mfma(f0, sf, z4), a1 = mfma(f1, sf, z4);
          if (c == 0) {
            stsf4(L, L_MODP + v * 256 + hh * 128 + g * 32, a0);
            stsf4(L, L_MODP + v * 256 + hh * 128 + g * 32 + 16, a1);
          }
        }
        asm volatile("s_waitcnt lgkmcnt(0)" ::: "memory");
        *(volatile LAS u32*)(L + L_ARR + v * 4) = tag;
        if (v == 0) {
          while ((__builtin_amdgcn_ballot_w64(*(volatile LAS u32*)(L + L_ARR + (lane < 7 ? lane : 0) * 4) != tag) != 0ull) & (budget > 0)) --budget;
          asm volatile("" ::: "memory");
          float s0 = 0.f;
#pragma unroll
          for (int k = 0; k < 7; ++k) s0 += *(LAS float*)(L + L_MODP + k * 256 + (lane >> 3) * 32 + (lane & 1) * 16 + (lane & 6) * 2);
          st64c(v2u{asu(s0), tag}, rws, WS_MU + (uq * 64 + lane) * 16 + ukh * 8, 0);
        }
      }
    }
    STAMP(1);
    int xsl[12], xrow[12];
    xsl[0] = v + 7 * ((rank * 3) >> 1);
#pragma unroll
    for (int i = 1; i < 12; ++i) xsl[i] = xsl[i - 1] + 7 >= 84 ? v : xsl[i - 1] + 7;
#pragma unroll
    for (int i = 0; i < 12; ++i) xrow[i] = xsl[i] < 78 ? xsl[i] + 2 : xsl[i] & 1;
    v4u xr[12];
#pragma unroll
    for (int i = 0; i < 12; ++i) {
      int tok = t0 - 16 + xrow[i];
      tok = tok < 0 ? 0 : tok;
      xr[i] = ld128(rx, lane < 56 ? l16 : OOB, tok * 896);
    }
    if (w == 7 && packer) {
      const int q = rank * 28 + pk_;
      const u64 wa = (u64)pwo, wm = (u64)pwm;
      dma4(v4u{(u32)wa, (u32)(wa >> 32) & 0xFFFFu, (u32)(D * D * 2), 0x00020000u}, lane < 14 ? q * 1792 + lane * 128 : OOB, L_DUMMY);
      dma4(v4u{(u32)wm, (u32)(wm >> 32) & 0xFFFFu, (u32)(D * 6 * D * 2), 0x00020000u},
           lane < 56 ? (2 * q + (lane >= 28)) * 5376 + 1792 + (lane >= 28 ? lane - 28 : lane) * 128 : OOB, L_DUMMY);
    }
    SB();
    f32x2 t[12][4];
    {
      float sm[12], sq[12];
#pragma unroll
      for (int i = 0; i < 12; ++i) {
        sm[i] = sq[i] = 0.f;
#pragma unroll
        for (int e = 0; e < 4; ++e) {
          sm[i] = dot2(xr[i][e], 0x3F803F80u, sm[i]);
          sq[i] = dot2(xr[i][e], xr[i][e], sq[i]);
        }
      }
      STAMP(2);
      float rs_[3], nn[3];
#pragma unroll
      for (int q = 0; q < 3; ++q) {
        const float m = rowsum(fold16(fold32(sm[4 * q], sm[4 * q + 2]), fold32(sm[4 * q + 1], sm[4 * q + 3]))) * (1.f / D);
        const float vr = rowsum(fold16(fold32(sq[4 * q], sq[4 * q + 2]), fold32(sq[4 * q + 1], sq[4 * q + 3]))) * (1.f / D);
        rs_[q] = __builtin_amdgcn_rsqf(fmaxf(vr - m * m, 0.f) + 1e-5f);
        nn[q] = -m * rs_[q];
        int r = xsl[4 * q] + 7 * g;
        r = r >= 84 ? r - 84 : r;
        if (c == 0) *(LAS float*)(L + L_MU1 + (r + 2) * 4) = m;
      }
#pragma unroll
      for (int i = 0; i < 12; ++i) {
        const float r = asf(__builtin_amdgcn_readlane(asu(rs_[i >> 2]), 16 * (i & 3)));
        float nv = asf(__builtin_amdgcn_readlane(asu(nn[i >> 2]), 16 * (i & 3)));
        asm("" : "+v"(nv));
#pragma unroll
        for (int e = 0; e < 4; ++e) {
          t[i][e] = fma2(up2(xr[i][e]), f32x2{r, r}, f32x2{nv, nv});
          asm volatile("" : "+v"(t[i][e]));
        }
      }
    }
    STAMP(3);
    SB();
    if (v < 4) {
#pragma unroll
      for (int i = 0; i < 14; i += 2) slab2(v * 14 + i);
    }
    if (v == 6 && lane < 32) sts128(L, L_B + 57344 + l16, v4u{0u, 0u, 0u, 0u});
    if (v < 4) {
      const v4u sv = {SENT, SENT, SENT, SENT};
      st128c(sv, rws, l16, o_cl + (v * 16 + 2 * rank) * 1024);
      st128c(sv, rws, l16 + 1024, o_cl + (v * 16 + 2 * rank) * 1024);
      if (rank < 7) {
        st128c(sv, rws, l16, x_cl + (v * 14 + 2 * rank) * 1024);
        st128c(sv, rws, l16 + 1024, x_cl + (v * 14 + 2 * rank) * 1024);
        st128c(sv, rws, g == 0 ? l16 : OOB, x_cl + XS_OFF + (v * 7 + rank) * 256);
      }
    }
    while ((__builtin_amdgcn_readfirstlane(*(volatile LAS u32*)(L + L_GO)) != tag) & (budget > 0)) {
      __builtin_amdgcn_s_sleep(1);
      --budget;
    }
    asm volatile("" ::: "memory");
    STAMP(4);
#pragma unroll
    for (int n = 0; n < 4; ++n) wq_load(n);
    SB();
    if (lane < 56) {
      const f32x4 ca[2] = {ldsf4(L, L_PAIR + 1792 + 2 * l16), ldsf4(L, L_PAIR + 1808 + 2 * l16)}, sa[2] = {ldsf4(L, L_PAIR + 2 * l16), ldsf4(L, L_PAIR + 16 + 2 * l16)};
      const int base = L_A + ((lane >> 2) * 64 + (lane & 3) * 16) * 16, l7 = (lane & 7) * 16;
#pragma unroll
      for (int i = 0; i < 12; ++i) {
        v4u hv;
        hv[0] = pk2(fma2(t[i][0], ca[0].xy, sa[0].xy));
        hv[1] = pk2(fma2(t[i][1], ca[0].zw, sa[0].zw));
        hv[2] = pk2(fma2(t[i][2], ca[1].xy, sa[1].xy));
        hv[3] = pk2(fma2(t[i][3], ca[1].zw, sa[1].zw));
        sts128(L, base + (l7 ^ ((xrow[i] & 15) * 16)) + (xrow[i] >> 4) * 14336, hv);
      }
    }
    SB();
#pragma unroll
    for (int n = 4; n < NQW; ++n) wq_load(n);
    if (rank == 7) waitvm<NQW>();
  }
  bar();
  STAMP(5);
  if (rank == 7 && packer && w < 4) {
    const int wb = L_B + (8 * g + (c >> 2)) * 128 + ((w ^ ((c >> 3) | ((g & 1) << 1))) << 5) + (c & 3) * 8;
    f32x4 acc = z4;
#pragma unroll
    for (int s2 = 0; s2 < 2; ++s2) {
      v4u sf[7], af[7];
#pragma unroll
      for (int q = 0; q < 7; ++q) {
        const int s = 7 * s2 + q;
        const v2u lo = trread(L, wb + s * 4096), hi = trread(L, wb + s * 4096 + 512);
        af[q] = v4u{lo[0], lo[1], hi[0], hi[1]};
        sf[q] = lds128(L, L_SC + s * 64 + g * 16);
      }
#pragma unroll
      for (int q = 0; q < 7; ++q) acc = mfma(af[q], sf[q], acc);
    }
    st128c(v4u{pk(acc[0], acc[1]), tag, pk(acc[2], acc[3]), tag}, rws, c == 0 ? WS_MOD + pk_ * 256 + w * 64 + g * 16 : OOB, 0);
  }

  v4u w1f[28];
  const int w1o = WS_P1 + rank * 200704 + w * 28672, w1v = w < 7 ? l16 : OOB;
  auto w1_load = [&](int n) LAM { w1f[n] = ld128c(rws, w1v + (n & 3) * 1024, w1o + (n >> 2) * 4096); };
  f32x4 akv[5], aq[4];
  const int fbo = w != 0 ? OOB : WS_FB + cl * 128 + (lane & 31) * 4;
  u32 fb;
  {
    const int hb0 = L_A + (g * 16 + (c ^ g)) * 16, hb1 = L_A + (g * 16 + (c ^ (4 + g))) * 16;
    const int ad[2][2] = {{(kh ? hb1 : hb0) + kh * 7168, (kh ? hb0 : hb1) + kh * 7168}, {(kh ? hb0 : hb1) + (1 - kh) * 7168, (kh ? hb1 : hb0) + (1 - kh) * 7168}};
#pragma unroll
    for (int r = 0; r < 5; ++r) akv[r] = f32x4{0.f, 0.f, 0.f, 0.f};
#pragma unroll
    for (int r = 0; r < 4; ++r) aq[r] = f32x4{0.f, 0.f, 0.f, 0.f};
    v4u hf[HFD];
    auto h_read = [&](int n) LAM { const int s = n / 5, r = n % 5; return lds128(L, ad[s & 1][(s >> 1) & 1] + r * 14336 + (s >> 1) * 1024); };
    u32 f1;
    const int f1o = lane < 14 ? WS_FA + rank * 1024 + (2 * lane + (w >> 2)) * 32 + 4 : OOB;
    const u64 f1m = w < 7 ? 0x3FFFull : 0ull;
#pragma unroll
    for (int n = 0; n < HFD; ++n) hf[n] = h_read(n);
    SB();
#pragma unroll
    for (int s = 0; s < 14; ++s) {
#pragma unroll
      for (int r = 0; r < 5; ++r) {
        akv[r] = mfma(wqf[3 * (s >> 1) + 2 * (s & 1)], hf[(5 * s + r) % HFD], akv[r]);
        if (!(s & 1) && r > 0) aq[r - 1] = mfma(wqf[3 * (s >> 1) + 1], hf[(5 * s + r) % HFD], aq[r - 1]);
        if (5 * s + r + HFD < 70) hf[(5 * s + r) % HFD] = h_read(5 * s + r + HFD);
        if (r == 0 && NQW + 3 * (s >> 1) + 2 * (s & 1) < 21) wq_load(NQW + 3 * (s >> 1) + 2 * (s & 1));
        if (r == 3 && !(s & 1) && NQW + 3 * (s >> 1) + 1 < 21) wq_load(NQW + 3 * (s >> 1) + 1);
        if (s == 0 && r == 4) st32c(tag, rws, lane != 0 || w > 4 ? OOB : w == 0 ? WS_FA + rank * 1024 + pk_ * 32 + 8 : WS_FB + cl * 128 + rank * 16 + (w - 1) * 4, 0);
        if (s == 11 && r == 0) fb = ld32c(rws, fbo, 0);
        if (s == 8 && r == 2) f1 = ld32c(rws, f1o, 0);
        if (s == 11 && r == 4) {
          while (((__builtin_amdgcn_ballot_w64(f1 != tag) & f1m) != 0ull) & (budget > 0)) {
            asm volatile("" ::: "memory");
            __builtin_amdgcn_s_sleep(PSLEEP);
      f1 = ld32c(rws, f1o, 0);
            --budget;
          }
          asm volatile("" ::: "memory");
        }
        if (s >= 12 && (r == 0 || r == 3)) w1_load(2 * (s - 12) + (r ? 1 : 0));
        SB();
      }
    }
  }
  if (w == 0) {
    while ((__builtin_amdgcn_ballot_w64(fb != tag) != 0ull) & (budget > 0)) {
      asm volatile("" ::: "memory");
      __builtin_amdgcn_s_sleep(PSLEEP);
      fb = ld32c(rws, fbo, 0);
      --budget;
    }
  }
  const u64 wsa = (u64)pws;
  const v4u rsw = {(u32)wsa, (u32)(wsa >> 32) & 0xFFFFu, rank < 7 ? (u32)WS_END : 0u, 0x00020000u};
  f32x4 bias[2];
  {
    const int tq = t0 + 16 * (w >> 1) + c;
#pragma unroll
    for (int e = 0; e < 8; ++e) {
      const int delta = c + (e < 4 ? 16 : 0) - 4 * g - (e & 3);
      bias[e >> 2][e & 3] = ((delta & ~14) == 0 && delta <= tq) ? 0.f : -1.0e30f;
    }
    asm volatile("" : "+v"(bias[0]), "+v"(bias[1]));
  }
  STAMP(6);
  bar();
  const int mmo = w < 7 ? WS_MOD + w * 1024 + l16 : WS_FA + rank * 1024 + (lane & 31) * 32;
  v4u mm = ld128c(rws, mmo, 0);
#pragma unroll
  for (int n = 4; n < 8; ++n) w1_load(n);
  SB();
  {
    const int qb = L_A + ((w >> 1) & 1) * QK_HD + l16;
#pragma unroll
    for (int r = 0; r < 5; ++r) sts64(L, qb + (kh * 5 + r) * 1024 + (w & 1) * 8, v2u{pk(akv[r][0], akv[r][1]), pk(akv[r][2], akv[r][3])});
#pragma unroll
    for (int r = 0; r < 4; ++r) stsf4(L, qb + (10 + kh * 8 + 2 * r + (w & 1)) * 1024, aq[r]);
  }
  bar();
  {
    const int rt = w >> 1, hd = w & 1;
    const int qb = L_A + hd * QK_HD + l16 + rt * 1024;
    const int qq = L_A + hd * QK_HD + l16 + (10 + 2 * rt) * 1024;
    const f32x4 qa = ldsf4(L, qq) + ldsf4(L, qq + 8192), qc = ldsf4(L, qq + 1024) + ldsf4(L, qq + 9216);
    const v4u k0 = lds128(L, qb), k1 = lds128(L, qb + 1024);
    const int vb = L_A + hd * QK_HD + (5 + rt) * 1024 + ((c & 3) * 16 + 4 * g + (c >> 2)) * 16;
    const v2u v00 = trread(L, vb), v01 = trread(L, vb + 8), v10 = trread(L, vb + 1024), v11 = trread(L, vb + 1032);
    const v4u qf = {pk(qa[0], qa[1]), pk(qa[2], qa[3]), pk(qc[0], qc[1]), pk(qc[2], qc[3])};
    const f32x4 z = {0.f, 0.f, 0.f, 0.f};
    const f32x4 s0 = mfma(k0, qf, bias[0]), s1 = mfma(k1, qf, bias[1]);
#pragma unroll
    for (int n = 8; n < 12; ++n) w1_load(n);
    SB();
    const float sc = 0.18898223650461362f * 1.44269504088896341f;
    const float mx = -sc * gmax(fmaxf(fmaxf(fmaxf(s0[0], s0[1]), fmaxf(s0[2], s0[3])), fmaxf(fmaxf(s1[0], s1[1]), fmaxf(s1[2], s1[3]))));
    float sv[8];
    float sum = 0.f;
#pragma unroll
    for (int e = 0; e < 8; ++e) {
      sv[e] = __builtin_amdgcn_exp2f(vfma(e < 4 ? s0[e & 3] : s1[e & 3], sc, mx));
      sum += sv[e];
    }
    const float inv = __builtin_amdgcn_rcpf(gsum(sum));
    v4u pf;
#pragma unroll
    for (int i = 0; i < 4; ++i) pf[i] = pk(sv[2 * i] * inv, sv[2 * i + 1] * inv);
    const f32x4 o0 = mfma(v4u{v00[0], v00[1], v10[0], v10[1]}, pf, z), o1 = mfma(v4u{v01[0], v01[1], v11[0], v11[1]}, pf, z);
    v4u of;
#pragma unroll
    for (int i = 0; i < 4; ++i) of[i] = canon(pk(o0[i], o1[i]));
    st128c(of, rws, l16, o_cl + (rt * 16 + 2 * rank + hd) * 1024);
  }
  STAMP(7);
  if (w < 7) {
    while ((__builtin_amdgcn_ballot_w64((mm[1] != tag) | (mm[3] != tag)) != 0ull) & (budget > 0)) {
      asm volatile("" ::: "memory");
      __builtin_amdgcn_s_sleep(PSLEEP);
      mm = ld128c(rws, mmo, 0);
      --budget;
    }
    const int q = w * 64 + lane;
    const float one = (q >= 224 && q < 336) ? 1.f : 0.f;
    sts64(L, L_GA + (lane & ~7) * 8 + (lane & 3) * 16 + (lane & 4) * 2 + w * 512,
          v2u{pk(one + lo16(mm[0]), one + hi16(mm[0])), pk(one + lo16(mm[2]), one + hi16(mm[2]))});
  } else {
    while ((__builtin_amdgcn_ballot_w64(mm[2] != tag) != 0ull) & (budget > 0)) {
      asm volatile("" ::: "memory");
      __builtin_amdgcn_s_sleep(PSLEEP);
      mm = ld128c(rws, mmo, 0);
      --budget;
    }
  }
  SB();
  const int rt = w & 3;
  v4u x1p = {0u, 0u, 0u, 0u};
  if (rank < 7) {
    int wb[4];
#pragma unroll
    for (int x = 0; x < 4; ++x) wb[x] = L_B + (kh * 224 + 8 * g + (c >> 2)) * 128 + ((x ^ ((c >> 3) | ((g & 1) << 1))) << 5) + (c & 3) * 8;
    auto wo_read = [&](int q) LAM {
      const int hs = q >> 2, ct = q & 3;
      const v2u lo = trread(L, wb[ct ^ (((7 * hs) >> 1) & 1) * 2] + 28 * hs * 128);
      const v2u hi = trread(L, wb[ct ^ (((7 * hs + 1) >> 1) & 1) * 2] + (28 * hs + 4) * 128);
      return v4u{lo[0], lo[1], hi[0], hi[1]};
    };
    v4u wo[16];
#pragma unroll
    for (int q = 0; q < 16; ++q) wo[q] = wo_read(q);
    bar();
    const int vo = rank * 128 + kh * 64 + g * 16;
    const v4u ga = lds128(L, L_GA + vo), cb = lds128(L, L_CB + vo);
    const float mu1 = *(LAS float*)(L + L_MU1 + (16 + 16 * rt + c) * 4);
    v4u of[8];
    const int ovo = o_cl + (rt * 16 + 8 * kh) * 1024 + l16;
    issue<8>(of, rws, ovo);
    const int xo = c * 896 + g * 8 + (t0 + 16 * rt) * 896 + rank * 128 + kh * 64;
    const v2u xr0 = ld64(rx, xo, 0), xr1 = ld64(rx, xo + 32, 0);
    SB();
    settle<8>(of, rws, ovo, budget);
    STAMP(8);
    f32x4 acc[4];
#pragma unroll
    for (int q = 0; q < 4; ++q) acc[q] = f32x4{0.f, 0.f, 0.f, 0.f};
#pragma unroll
    for (int q = 0; q < 32; ++q) {
      acc[q & 3] = mfma(wo[q & 15], of[q >> 2], acc[q & 3]);
      if (q < 16) wo[q] = wo_read(16 + q);
      if (q % 2 == 1 && 12 + q / 2 < N1C) w1_load(12 + q / 2);
      SB();
    }
#pragma unroll
    for (int q = 0; q < 4; ++q) stsf4(L, L_A + (w * 4 + q) * 1024 + l16, acc[q]);
    bar();
    const int pb = L_A + (rt * 4 + 2 * kh) * 1024 + l16;
    const f32x4 a0 = ldsf4(L, pb) + ldsf4(L, pb + 16384), a1 = ldsf4(L, pb + 1024) + ldsf4(L, pb + 17408);
    const v4u xres = {xr0[0], xr0[1], xr1[0], xr1[1]};
    v4u xp;
    f32x2 s1v = {0.f, 0.f}, s2v = {0.f, 0.f};
#pragma unroll
    for (int i = 0; i < 4; ++i) {
      x1p[i] = pk2(fma2(up2(ga[i]), i == 0 ? a0.xy : i == 1 ? a0.zw : i == 2 ? a1.xy : a1.zw, up2(xres[i])));
      const f32x2 d = up2(x1p[i]) - f32x2{mu1, mu1};
      s1v += d;
      s2v = fma2(d, d, s2v);
      xp[i] = canon(pk2(d * up2(cb[i])));
    }
    float s1 = s1v.x + s1v.y, s2 = s2v.x + s2v.y;
    st128c(xp, rws, l16, x_cl + (rt * 14 + 2 * rank + kh) * 1024);
    s1 = gsum(s1);
    s2 = gsum(s2);
    st64c(v2u{canon(asu(s1)), canon(asu(s2))}, rws, g == 0 ? l16 : OOB, x_cl + XS_OFF + (rt * 7 + rank) * 256 + kh * 8);
  } else {
    bar();
#pragma unroll
    for (int n = 12; n < N1C; ++n) w1_load(n);
    bar();
  }
  STAMP(9);
  v4u w2f[28];
  f32x4 u0 = z4, u1 = z4;
  const int w2o = WS_P2 + rank * 229376 + w * 28672, w2v = rank < 7 ? l16 : OOB;
  auto w2_load = [&](int n) LAM { w2f[n] = ld128c(rws, w2v + (n & 3) * 1024, w2o + (n >> 2) * 4096); };
  constexpr int L_W2 = 57344, NST = 28 - N2B;
  static_assert(N2B == 16, "counted wait in the W1 GEMM");
  static_assert(L_W2 + 8 * NST * 1024 <= L_GA, "W2 staging");
  const v4u rsh = {(u32)wsa, (u32)(wsa >> 32) & 0xFFFFu, (u32)WS_END, 0x00020000u};
  {
    v4u xf[7], xs[1];
    const int pr = 4 * kh + g;
    const int xo = x_cl + (rt * 14 + 7 * kh) * 1024 + l16, so = pr < 7 ? x_cl + XS_OFF + (rt * 7 + pr) * 256 + c * 16 : OOB;
    const int vb = ((c & 1) ? L_SB : L_CB) + g * 16;
    {
      v4u bfv[N1C / 2];
#pragma unroll
      for (int s = 0; s < N1C / 2; ++s) bfv[s] = lds128(L, vb + s * 64);
      SB();
#pragma unroll
      for (int s = 0; s < N1C / 2; ++s) {
        u0 = mfma(w1f[2 * s], bfv[s], u0);
        u1 = mfma(w1f[2 * s + 1], bfv[s], u1);
      }
    }
    bar();
    issue<7>(xf, rws, xo);
    issue<1>(xs, rws, so);
    SB();
    settle<7>(xf, rws, xo, budget);
    settle<1>(xs, rws, so, budget);
#pragma unroll
    for (int n = N1C; n < 28; ++n) w1_load(n);
    SB();
#pragma unroll
    for (int i = 0; i < 7; ++i) sts128(L, L_A + (rt * 14 + 7 * kh + i) * 1024 + l16, xf[i]);
    const float s1 = gsum(asf(xs[0][0]) + asf(xs[0][2])), s2 = gsum(asf(xs[0][1]) + asf(xs[0][3]));
    if (g == 0) sts64(L, L_ST + ((rt * 2 + kh) * 16 + c) * 8, v2u{asu(s1), asu(s2)});
    if (tid == 0) *(LAS u32*)(L + L_CNT) = 0u;
    SB();
#pragma unroll
    for (int n = 0; n < 4; ++n) w2_load(n);
  }
  STAMP(10);
  bar();
  STAMP(11);
  const int fho = lane == 0 ? WS_FH + cl * 1024 + rank * 128 + w * 4 : OOB;
  const int fwo = (lane < 32 && rank < 7) ? WS_FH + cl * 1024 + w * 128 + lane * 4 : OOB;
  u32 fh;
  v4u hf[4][7];
  const int ho = rank < 7 ? h_cl + w * 28672 + l16 : OOB;
  const u64 live = rank < 7 ? ~0ull : 0ull;
  auto need = [&](u64 mask) LAM {
    while (((__builtin_amdgcn_ballot_w64(fh != tag) & mask & live) != 0ull) & (budget > 0)) {
      asm volatile("" ::: "memory");
      __builtin_amdgcn_s_sleep(PSLEEP);
      fh = ld32c(rws, fwo, 0);
      --budget;
    }
    asm volatile("" ::: "memory");
  };
  if (w < 7) {
    v4u xf[4];
#pragma unroll
    for (int i = 0; i < 4; ++i) xf[i] = lds128(L, L_A + i * 1024 + l16);
    const int ub = L_UV + w * 256 + g * 64;
    const int vb = ((c & 1) ? L_SB : L_CB) + g * 16;
    f32x4 uu[2], vv[2];
    SB();
    const float nl2e = -1.44269504f;
    auto flag = [&](int r) LAM {
      st32c(tag, rws, fho, r * 32);
      if (lane == 0) (void)__hip_atomic_fetch_add((LAS u32*)(L + L_CNT), 1u << (8 * r), __ATOMIC_RELAXED, __HIP_MEMORY_SCOPE_WORKGROUP);
    };
#pragma unroll
    for (int r = 0; r < 4; ++r) {
      f32x4 a[2] = {z4, z4};
      v2u sta, stb;
#pragma unroll
      for (int s = 0; s < 14; ++s) {
        const int n = r * 14 + s;
        a[0] = mfma(w1f[2 * s], xf[n & 3], a[0]);
        a[1] = mfma(w1f[2 * s + 1], xf[n & 3], a[1]);
        if (n + 4 < 56) xf[n & 3] = lds128(L, L_A + (n + 4) * 1024 + l16);
        if (s == 8) {
          sta = *(LAS v2u*)(L + L_ST + (r * 32 + c) * 8);
          stb = *(LAS v2u*)(L + L_ST + (r * 32 + 16 + c) * 8);
        }
        if (r == 0 && s >= N1C / 2) {
          const v4u bv = lds128(L, vb + s * 64);
          u0 = mfma(w1f[2 * s], bv, u0);
          u1 = mfma(w1f[2 * s + 1], bv, u1);
        }
        if (n >= 4 && n % 2 == 0 && 4 + (n - 4) / 2 < N2B) w2_load(4 + (n - 4) / 2);
        if (r > 0 && s == 11) {
          if (r == 1) waitvm<6>();
          else waitvm<0>();
          flag(r - 1);
        }
        if (r == 3 && s == 12) fh = ld32c(rws, fwo, 0);
        SB();
      }
      if (r == 0) {
        const float sc = (c & 1) ? nl2e : 1.f;
        if (c < 2) {
          stsf4(L, ub + c * 32, u0 * sc);
          stsf4(L, ub + c * 32 + 16, u1 * sc);
        }
        uu[0] = ldsf4(L, ub);
        uu[1] = ldsf4(L, ub + 16);
        vv[0] = ldsf4(L, ub + 32);
        vv[1] = ldsf4(L, ub + 48);
      }
      const float mu = (asf(sta[0]) + asf(stb[0])) * (1.f / D);
      const float var = fmaxf((asf(sta[1]) + asf(stb[1])) * (1.f / D) - mu * mu, 0.f);
      const float rsl = nl2e * __builtin_amdgcn_rsqf(var + 1e-5f);
      float y[8];
#pragma unroll
      for (int e = 0; e < 8; ++e) {
        const float z = vfma(vfma(-mu, uu[e & 1][e >> 1], a[e & 1][e >> 1]), rsl, vv[e & 1][e >> 1]);
        y[e] = z * __builtin_amdgcn_rcpf(vfma(__builtin_amdgcn_exp2f(z), nl2e, nl2e));
      }
      const v4u hv = {pkn(y[0], y[1]), pkn(y[2], y[3]), pkn(y[4], y[5]), pkn(y[6], y[7])};
      st128c(hv, rws, l16, h_cl + (rank * 28 + w + r * 7) * 1024);
      SB();
    }
    if (rank == 7) {
      drain();
      flag(3);
    }
  } else {
#pragma unroll
    for (int n = 4; n < N2B; ++n) w2_load(n);
    const int own = h_cl + rank * 28672 + l16;
    int warmed = 0;
#pragma unroll 1
    for (int ww = 0; ww < 8; ++ww) {
#pragma unroll
      for (int q = 0; q < NST; ++q) dma1k(rsw, l16, WS_P2 + rank * 229376 + (ww * 28 + N2B + q) * 1024, L_W2 + (ww * NST + q) * 1024);
      if (warmed < 3 && ((__builtin_amdgcn_readfirstlane(*(volatile LAS u32*)(L + L_CNT)) >> (8 * warmed)) & 255u) == 7u) {
#pragma unroll
        for (int i = 0; i < 7; ++i) dma1k(rsh, own + i * 1024, warmed * 7168, L_DUMMY);
        ++warmed;
      }
    }
    fh = ld32c(rws, fwo, 0);
    drain();
  }
  STAMP(12);
  if (rank < 7) {
    need(0x7F7Full);
    issue<7>(hf[0], rws, ho);
    issue<7>(hf[1], rws, ho + 7168);
    fh = ld32c(rws, fwo, 0);
    bar();
#pragma unroll
    for (int n = 0; n < NST; ++n) w2f[N2B + n] = lds128(L, L_W2 + (w * NST + n) * 1024 + l16);
    waitvm<15>();
    st32c(tag, rws, fho, 96);
#pragma unroll
    for (int r = 0; r < 4; ++r) {
      if (r == 1 || r == 2) {
        need(0x7Full << (8 + 8 * r));
        issue<7>(hf[r + 1], rws, ho + (r + 1) * 7168);
        if (r == 1) fh = ld32c(rws, fwo, 0);
      }
      SB();
      f32x4 acc[4];
#pragma unroll
      for (int q = 0; q < 4; ++q) acc[q] = z4;
#pragma unroll
      for (int s = 0; s < 7; ++s) {
#pragma unroll
        for (int q = 0; q < 4; ++q) acc[q] = mfma(w2f[4 * s + q], hf[r][s], acc[q]);
      }
      SB();
      if (r == 1) bar();
#pragma unroll
      for (int q = 0; q < 4; ++q) stsf4(L, ((r * 8 + w) * 4 + q) * 1024 + l16, acc[q]);
      SB();
    }
    STAMP(13);
    const v4u gb = lds128(L, L_GB + rank * 128 + kh * 64 + g * 16);
    bar();
    f32x4 pa[8][2];
#pragma unroll
    for (int p = 0; p < 8; ++p) {
      pa[p][0] = ldsf4(L, ((rt * 8 + p) * 4 + 2 * kh) * 1024 + l16);
      pa[p][1] = ldsf4(L, ((rt * 8 + p) * 4 + 2 * kh + 1) * 1024 + l16);
    }
    SB();
    f32x4 a0 = pa[0][0], a1 = pa[0][1];
#pragma unroll
    for (int p = 1; p < 8; ++p) {
      a0 += pa[p][0];
      a1 += pa[p][1];
    }
    v4u ov;
#pragma unroll
    for (int i = 0; i < 4; ++i) {
      ov[i] = pkn(vfma(lo16(gb[i]), a0[i], lo16(x1p[i])), vfma(hi16(gb[i]), a1[i], hi16(x1p[i])));
    }
    const rsrc_t rout = mkrs(pout, S * D * 2);
    const int oo = c * 896 + g * 8 + (t0 + 16 * rt) * 896 + rank * 128 + kh * 64;
    __builtin_amdgcn_raw_buffer_store_b64(v2u{ov[0], ov[1]}, rout, oo, 0, 0);
    __builtin_amdgcn_raw_buffer_store_b64(v2u{ov[2], ov[3]}, rout, oo + 32, 0, 0);
  } else if (cl == 0 && w == 7) {
    for (;;) {
      asm volatile("" ::: "memory");
      bool ok = true;
#pragma unroll
      for (int i = 0; i < 4; ++i) {
        const v4u f = ld128c(rws, l16 + i * 1024, WS_FB);
        ok = ok & (f[0] == tag) & (f[1] == tag) & (f[2] == tag) & (f[3] == tag);
      }
      --budget;
      if ((__builtin_amdgcn_ballot_w64(!ok) == 0ull) | (budget <= 0)) break;
      __builtin_amdgcn_s_sleep(8);
    }
    if (lane == 0) st32c(tag, rws, WS_EPOCH, 0);
  }
  STAMP(14);
  asm volatile(".Lfk_pc1:");
}
}

extern "C" void kernel_launch(void* const* d_in, const int* in_sizes, int n_in, void* d_out, int out_size, void* d_ws,
                              size_t ws_size, hipStream_t stream) {
  (void)hipFuncSetAttribute((const void*)fk::fused, hipFuncAttributeMaxDynamicSharedMemorySize, fk::L_END);
  fk::fused<<<256, 512, fk::L_END, stream>>>(d_in[0], d_in[1], d_in[2], d_in[4], d_in[5], d_in[6], d_in[7], d_out, d_ws);
}
```

```cpp
#include <hip/hip_runtime.h>

typedef unsigned u32;
typedef unsigned long long u64;
typedef u32 v4u __attribute__((ext_vector_type(4)));
typedef u32 v2u __attribute__((ext_vector_type(2)));
typedef float f32x4 __attribute__((ext_vector_type(4)));
typedef float f32x2 __attribute__((ext_vector_type(2)));
typedef short bf16x8 __attribute__((ext_vector_type(8)));
typedef __bf16 bf16x2 __attribute__((ext_vector_type(2)));
typedef __amdgpu_buffer_rsrc_t rsrc_t;
#define LAS __attribute__((address_space(3)))
#define SB() __builtin_amdgcn_sched_barrier(0)
#define NOPK
#define DEV __device__ __forceinline__ NOPK
#define LAM __attribute__((always_inline)) NOPK
#define fmaxf __builtin_fmaxf

namespace fk {
constexpr int S = 2048, D = 448, F = 1792;
constexpr int WS_EPOCH = 0;
constexpr int WS_FA = 81920;
constexpr int WS_FB = 8192;
constexpr int WS_FH = 32768;
constexpr int WS_MOD = 16384;
constexpr int WS_MU = 65536;
constexpr int WS_O = 131072;
constexpr int WS_X = WS_O + 32 * 65536;
constexpr int XS_OFF = 57344;
constexpr int WS_H = WS_X + 32 * 65536;
constexpr int H_CL = 8 * 4 * 7 * 1024;
constexpr int WS_PQ = WS_H + 32 * H_CL;
constexpr int WS_PO = WS_PQ + 8 * 168 * 1024;
constexpr int WS_P2 = WS_PO + 8 * 65536;
constexpr int WS_P1 = WS_P2 + 8 * 224 * 1024;
constexpr int WS_END = WS_P1 + 8 * 196 * 1024;
constexpr int L_A = 0;
constexpr int L_B = 71680;
constexpr int L_PAIR = 137216;
constexpr int L_MODP = 141312;
constexpr int L_SC = 143360;
constexpr int L_XR = 146432;
constexpr int L_MU1 = 154688;
constexpr int L_GA = 155648, L_SB = L_GA + 896, L_CB = L_SB + 896, L_GB = L_CB + 896;
constexpr int L_ST = L_GB + 896;
constexpr int L_UV = L_ST + 1024;
constexpr int L_CNT = L_UV + 1792;
constexpr int L_GO = L_CNT + 16, L_ARR = L_CNT + 32;
constexpr int L_DUMMY = L_CNT + 64;
constexpr int L_END = L_DUMMY + 1024;
static_assert(L_END <= 163840, "LDS");
constexpr int QK_HD = 26 * 1024;
constexpr u32 SENT = 0xFFFFFFFFu;
constexpr int SPIN_CAP = 1 << 16;
constexpr int OOB = 0x40000000;
constexpr int CODE_SLICES = 8;
#define PSLEEP 6
#define NPF 14
#define HFD 8
#define NQW 4
#define N1B 12
#define N1C 22
#define N2A 8
#define N2B 16

DEV float asf(u32 x) { return __builtin_bit_cast(float, x); }
DEV u32 asu(float x) { return __builtin_bit_cast(u32, x); }
DEV float lo16(u32 x) { return asf(x << 16); }
DEV float hi16(u32 x) { return asf(x & 0xFFFF0000u); }
DEV u32 pk(float a, float b) {
  f32x2 v = {a, b};
  return __builtin_bit_cast(u32, __builtin_convertvector(v, bf16x2));
}
DEV u32 pk2(f32x2 v) { return __builtin_bit_cast(u32, __builtin_convertvector(v, bf16x2)); }
DEV u32 pkn(float a, float b) {
  asm("" : "+v"(a));
  asm("" : "+v"(b));
  return pk(a, b);
}
DEV f32x2 up2(u32 x) { return f32x2{lo16(x), hi16(x)}; }
DEV f32x2 fma2(f32x2 a, f32x2 b, f32x2 c) { return __builtin_elementwise_fma(a, b, c); }
DEV u32 canon(u32 v) { return v < 0xFFFFFFFEu ? v : 0xFFFFFFFEu; }
DEV f32x4 mfma(v4u a, v4u b, f32x4 c) {
  return __builtin_amdgcn_mfma_f32_16x16x32_bf16(__builtin_bit_cast(bf16x8, a), __builtin_bit_cast(bf16x8, b), c, 0, 0, 0);
}
DEV float dot2(u32 a, u32 b, float c) {
  return __builtin_amdgcn_fdot2_f32_bf16(__builtin_bit_cast(bf16x2, a), __builtin_bit_cast(bf16x2, b), c, false);
}
DEV rsrc_t mkrs(const void* p, int bytes) { return __builtin_amdgcn_make_buffer_rsrc((void*)p, 0, bytes, 0x00020000); }
DEV u32 ld32(rsrc_t r, int v, int s) { return __builtin_amdgcn_raw_buffer_load_b32(r, v, s, 0); }
DEV v4u ld128(rsrc_t r, int v, int s) { return __builtin_amdgcn_raw_buffer_load_b128(r, v, s, 0); }
DEV u32 ld16(rsrc_t r, int v, int s) { return __builtin_amdgcn_raw_buffer_load_b16(r, v, s, 0); }
DEV v2u ld64(rsrc_t r, int v, int s) { return __builtin_amdgcn_raw_buffer_load_b64(r, v, s, 0); }
DEV v2u ld64c(rsrc_t r, int v, int s) { return __builtin_amdgcn_raw_buffer_load_b64(r, v, s, 16); }
DEV u32 ld32c(rsrc_t r, int v, int s) { return __builtin_amdgcn_raw_buffer_load_b32(r, v, s, 16); }
DEV v4u ld128c(rsrc_t r, int v, int s) { return __builtin_amdgcn_raw_buffer_load_b128(r, v, s, 16); }
DEV void st128c(v4u x, rsrc_t r, int v, int s) { __builtin_amdgcn_raw_buffer_store_b128(x, r, v + s, 0, 16); }
DEV void st64c(v2u x, rsrc_t r, int v, int s) { __builtin_amdgcn_raw_buffer_store_b64(x, r, v, s, 16); }
DEV void st32c(u32 x, rsrc_t r, int v, int s) { __builtin_amdgcn_raw_buffer_store_b32(x, r, v, s, 16); }
DEV v4u lds128(LAS char* L, int off) { return *(LAS v4u*)(L + off); }
DEV f32x4 ldsf4(LAS char* L, int off) { return *(LAS f32x4*)(L + off); }
DEV void sts128(LAS char* L, int off, v4u x) { *(LAS v4u*)(L + off) = x; }
DEV void stsf4(LAS char* L, int off, f32x4 x) { *(LAS f32x4*)(L + off) = x; }
DEV void sts64(LAS char* L, int off, v2u x) { *(LAS v2u*)(L + off) = x; }
typedef short v4s __attribute__((ext_vector_type(4)));
DEV v2u trread(LAS char* L, int off) { return __builtin_bit_cast(v2u, __builtin_amdgcn_ds_read_tr16_b64_v4i16((LAS v4s*)(L + off))); }
DEV void bar() {
  asm volatile("s_waitcnt lgkmcnt(0)" ::: "memory");
  __builtin_amdgcn_s_barrier();
  asm volatile("" ::: "memory");
}
DEV void drain() { asm volatile("s_waitcnt vmcnt(0)" ::: "memory"); }
template <int N>
DEV void waitvm() { asm volatile("s_waitcnt vmcnt(%0)" ::"n"(N) : "memory"); }
DEV void pin(v4u& x) { asm volatile("" : "+v"(x)); }
DEV void dma1k(v4u rs, int voff, int soff, int lds) {
  asm volatile("s_mov_b32 m0, %2\n\ts_nop 0\n\tbuffer_load_dwordx4 %0, %1, %3 offen sc1 lds" ::"v"(voff), "s"(rs), "s"(lds), "s"(soff) : "memory");
}
DEV void dma1kp(v4u rs, int voff, int soff, int lds) {
  asm volatile("s_mov_b32 m0, %2\n\ts_nop 0\n\tbuffer_load_dwordx4 %0, %1, %3 offen lds" ::"v"(voff), "s"(rs), "s"(lds), "s"(soff) : "memory");
}
DEV void dma4(v4u rs, int voff, int lds) {
  asm volatile("s_mov_b32 m0, %2\n\ts_nop 0\n\tbuffer_load_dword %0, %1, 0 offen lds" ::"v"(voff), "s"(rs), "s"(lds) : "memory");
}
DEV void xpose(const u32* r, v4u& f0, v4u& f1) {
#pragma unroll
  for (int i = 0; i < 4; ++i) {
    f0[i] = __builtin_amdgcn_perm(r[2 * i + 1], r[2 * i], 0x05040100u);
    f1[i] = __builtin_amdgcn_perm(r[2 * i + 1], r[2 * i], 0x07060302u);
  }
}
template <int CTRL>
DEV float dppadd(float x) {
  return x + asf(__builtin_amdgcn_update_dpp(0, asu(x), CTRL, 0xF, 0xF, true));
}
DEV float rowsum(float x) {
  x = dppadd<0x128>(x);
  x = dppadd<0x124>(x);
  x = dppadd<0x122>(x);
  return dppadd<0x121>(x);
}
DEV float fold32(float a, float b) {
  auto r = __builtin_amdgcn_permlane32_swap(asu(a), asu(b), false, false);
  return asf(r[0]) + asf(r[1]);
}
DEV float fold16(float a, float b) {
  auto r = __builtin_amdgcn_permlane16_swap(asu(a), asu(b), false, false);
  return asf(r[0]) + asf(r[1]);
}
DEV float gsum(float x) { x = fold16(x, x); return fold32(x, x); }
DEV float gmax(float x) {
  auto r = __builtin_amdgcn_permlane16_swap(asu(x), asu(x), false, false);
  x = fmaxf(asf(r[0]), asf(r[1]));
  auto q = __builtin_amdgcn_permlane32_swap(asu(x), asu(x), false, false);
  return fmaxf(asf(q[0]), asf(q[1]));
}
DEV u32 umax(u32 a, u32 b) { return a > b ? a : b; }
DEV u32 max4(v4u x, u32 m) {
  m = umax(umax(x[0], x[1]), m);
  return umax(umax(x[2], x[3]), m);
}
template <int N>
DEV void issue(v4u (&f)[N], rsrc_t rs, int voff) {
#pragma unroll
  for (int i = 0; i < N; ++i) f[i] = ld128c(rs, voff + (i & 3) * 1024, (i >> 2) * 4096);
}
template <int N>
DEV bool pending(const v4u (&f)[N]) {
  u32 m = 0;
#pragma unroll
  for (int i = 0; i < N; ++i) m = max4(f[i], m);
  return __builtin_amdgcn_ballot_w64(m == SENT) != 0ull;
}
DEV bool pend1(v4u f) { return __builtin_amdgcn_ballot_w64(max4(f, 0u) == SENT) != 0ull; }
template <int N>
DEV void settle(v4u (&f)[N], rsrc_t rs, int voff, int& budget) {
  if (pending<N>(f)) {
    bool again;
    do {
      __builtin_amdgcn_s_sleep(PSLEEP);
      asm volatile("" ::: "memory");
#pragma unroll
      for (int i = 0; i < N; ++i)
        if (pend1(f[i])) f[i] = ld128c(rs, voff + (i & 3) * 1024, (i >> 2) * 4096);
      --budget;
      again = pending<N>(f) & (budget > 0);
    } while (again);
  }
}
DEV float vfma(float a, float b, float c) { return __builtin_fmaf(a, b, c); }
DEV float vfma_s(float a, float sb, float c) { return __builtin_fmaf(a, sb, c); }
DEV float vmul(float a, float b) { return a * b; }
DEV bool all4(v4u f, u32 tag) { return __builtin_amdgcn_ballot_w64(!((f[0] == tag) & (f[1] == tag) & (f[2] == tag) & (f[3] == tag))) == 0ull; }
DEV void wait_flags(v4u f, rsrc_t rs, int voff, u32 tag, int& budget) {
  while (!all4(f, tag) & (budget > 0)) {
    __builtin_amdgcn_s_sleep(1);
    asm volatile("" ::: "memory");
    f = ld128c(rs, voff, 0);
    --budget;
  }
  asm volatile("" ::: "memory");
}
DEV float silu(float x) { return x * __builtin_amdgcn_rcpf(1.f + __builtin_amdgcn_exp2f(-1.44269504f * x)); }

#define STAMP(k) do { } while (0)

__global__ void __launch_bounds__(512, 2) NOPK
fused(const void* __restrict__ px, const void* __restrict__ pwqkv, const void* __restrict__ pwo, const void* __restrict__ pw1,
      const void* __restrict__ pw2, const void* __restrict__ pc, const void* __restrict__ pwm, void* pout, void* pws) {
  extern __shared__ __attribute__((aligned(16))) char smem[];
  LAS char* L = (LAS char*)smem;
  STAMP(0);
  const int tid = threadIdx.x, lane = tid & 63, w = __builtin_amdgcn_readfirstlane(tid >> 6);
  const int c = lane & 15, g = lane >> 4, l16 = lane * 16;
  const int b = blockIdx.x, xcd = b & 7, j = b >> 3, cl = xcd * 4 + (j >> 3), rank = j & 7, t0 = cl * 64;
  int budget = SPIN_CAP;

  u32 codepf;
  {
    u64 pcv;
    u32 clen;
    asm volatile("s_getpc_b64 %0\n.Lfk_pc0:\n\ts_mov_b32 %1, .Lfk_pc1-.Lfk_pc0" : "=s"(pcv), "=s"(clen));
    codepf = ld32(mkrs((const void*)pcv, w == 0 ? (int)clen : 0), lane * 128 + (j & (CODE_SLICES - 1)) * 8192, 0);
  }
  asm volatile("" ::"s"(px), "s"(pwqkv), "s"(pwo), "s"(pw1), "s"(pw2), "s"(pc), "s"(pwm), "s"(pout), "s"(pws));
  const rsrc_t rws = mkrs(pws, WS_END);
  const u32 tag = *(const __attribute__((address_space(4))) u32*)((const char*)pws + WS_EPOCH) + 1u;
  const int rr = (rank + xcd) & 7, pk_ = 4 * rr + (j >> 3);
  const bool packer = rr < 7;
  const f32x4 z4 = {0.f, 0.f, 0.f, 0.f};
  const rsrc_t rc = mkrs(pc, D * 2), rwm = mkrs(pwm, D * 6 * D * 2), rx = mkrs(px, S * D * 2);
  const int o_cl = WS_O + cl * 65536, x_cl = WS_X + cl * 65536, h_cl = WS_H + cl * H_CL;
  const int kh = w >> 2;
  v4u wqf[21];
  const int pe = WS_PQ + ((rank * 12 + 4 + w) * 14 + 7 * kh) * 1024, po = WS_PQ + ((rank * 12 + 4 + w) * 14 + 7 - 7 * kh) * 1024;
  const int pq = WS_PQ + ((rank * 12 + (w & 3)) * 14 + 7 * kh) * 1024;
  auto wq_load = [&](int n) LAM {
    const int i = n / 3, k = n - 3 * i;
    wqf[n] = ld128c(rws, l16 + (i & 3) * 1024, (k == 0 ? pe : k == 1 ? pq : po) + (i >> 2) * 4096);
  };
  auto slab2 = [&](int i) LAM {
    const u64 wa = rank < 7 ? (u64)pwo : (u64)pwm;
    const int st = rank < 7 ? 896 : 5376;
    const v4u rso = {(u32)wa, (u32)(wa >> 32) & 0xFFFFu, rank < 7 ? (u32)(D * D * 2) : packer ? (u32)(D * 6 * D * 2) : 0u, 0x00020000u};
    const int r8 = lane >> 3, ch = lane & 7;
    const int v0 = r8 * st + (rank < 7 ? rank * 128 : 1792 + pk_ * 128) + ((ch ^ (r8 & 2)) << 4), v1 = v0 ^ 64;
    dma1kp(rso, v0, i * 8 * st, L_B + i * 1024);
    dma1kp(rso, v1, (i + 1) * 8 * st, L_B + (i + 1) * 1024);
  };
  if (w == 0) {
    const int ks = pk_ >> 1;
    const int fa_own = lane == 0 ? WS_FA + rank * 1024 + pk_ * 32 : OOB;
    u32 rb[32];
    if (packer) {
      u32 ra[24];
      const rsrc_t rq = mkrs(pwqkv, D * 3 * D * 2), r1 = mkrs(pw1, D * F * 2);
      const int vo = c < 14 ? g * 8 * 2688 + c * 4 : OOB;
#pragma unroll
      for (int u = 0; u < 3; ++u) {
        const int pr = 3 * (pk_ & 1) + u, so = ks * 32 * 2688 + (pr >> 1) * 896 + (pr & 1) * 56 + rank * 112;
#pragma unroll
        for (int jj = 0; jj < 8; ++jj) ra[8 * u + jj] = ld32(rq, vo + (jj & 1) * 2688, so + (jj >> 1) * 5376);
      }
      SB();
#pragma unroll
      for (int u = 0; u < 3; ++u) {
        v4u f0, f1;
        xpose(ra + 8 * u, f0, f1);
        const int dst = WS_PQ + ((rank * 12 + (3 * (pk_ & 1) + u) * 2) * 14 + ks) * 1024;
        st128c(f0, rws, l16, dst);
        st128c(f1, rws, l16, dst + 14336);
      }
      SB();
      const int v1 = g * 4 * 3584 + c * 4, v1l = (pk_ & 1) ? OOB : v1, s1 = ks * 32 * 3584 + rank * 448 + (pk_ & 1) * 256;
#pragma unroll
      for (int i = 0; i < 32; ++i) rb[i] = ld32(r1, (i < 24 ? v1 : v1l) + (i & 1) * 3584 + (i >> 3) * 64, s1 + ((i & 2) + ((i >> 2) & 1) * 16) * 3584);
      waitvm<32>();
    }
    st32c(tag, rws, fa_own, 0);
    STAMP(1);
    if (!packer) {
      __builtin_amdgcn_s_sleep(64);
      const v4u sv = {SENT, SENT, SENT, SENT};
#pragma unroll 1
      for (int p = 0; p < 8; ++p) {
#pragma unroll
        for (int i = 0; i < 7; ++i) st128c(sv, rws, l16 + i * 1024, h_cl + p * 28672);
      }
    }
    u32 rd[32];
    if (packer) {
#pragma unroll
      for (int u = 0; u < 4; ++u) {
        v4u f0, f1;
        xpose(rb + 8 * u, f0, f1);
        const int dst = WS_P1 + rank * 200704 + ((4 * (pk_ & 1) + u) * 14 + ks) * 2048, vo1 = (u == 3 && (pk_ & 1)) ? OOB : l16;
        st128c(f0, rws, vo1, dst);
        st128c(f1, rws, vo1 + 1024, dst);
      }
      SB();
      const rsrc_t r2 = mkrs(pw2, F * D * 2);
      const int vo = g * 8 * 896 + rank * 128 + (c >> 2) * 8 + (c & 1) * 4 + (c & 2) * 16, so = pk_ * 64 * 896;
      if (rank < 7) {
#pragma unroll
        for (int i = 0; i < 32; ++i) rd[i] = ld32(r2, vo + (i & 3) * 896 + ((i >> 3) & 1) * 64, so + ((i >> 4) * 32 + (i & 4)) * 896);
      }
    }
    SB();
    u32 cv[7];
    if (rank == 7) {
#pragma unroll
      for (int i = 0; i < 7; ++i) cv[i] = ld16(rc, lane * 2 + i * 128, 0);
    }
    {
      v4u m[14];
      u32 fa;
      for (;;) {
        asm volatile("" ::: "memory");
#pragma unroll
        for (int i = 0; i < 14; ++i) m[i] = ld128c(rws, l16 + (i & 3) * 1024, WS_MU + (i >> 2) * 4096);
        fa = ld32c(rws, WS_FA + rank * 1024 + (lane & 31) * 32, 0);
        bool ok = fa == tag;
#pragma unroll
        for (int i = 0; i < 14; ++i) ok = ok & (m[i][1] == tag) & (m[i][3] == tag);
        --budget;
        if ((__builtin_amdgcn_ballot_w64(!ok) == 0ull) | (budget <= 0)) break;
        __builtin_amdgcn_s_sleep(PSLEEP);
      }
#pragma unroll
      for (int i = 0; i < 14; i += 2) {
        u32 p = pk(asf(m[i][0]) + asf(m[i][2]), asf(m[i + 1][0]) + asf(m[i + 1][2]));
        if (i >= 6) p = pk((i > 6 ? 1.f : 0.f) + lo16(p), 1.f + hi16(p));
        *(LAS float*)(L + L_PAIR + i * 256 + lane * 4) = lo16(p);
        *(LAS float*)(L + L_PAIR + (i + 1) * 256 + lane * 4) = hi16(p);
      }
      asm volatile("s_waitcnt lgkmcnt(0)" ::: "memory");
      *(volatile LAS u32*)(L + L_GO) = tag;
    }
    st32c(tag, rws, fa_own, 4);
    if (!packer) st32c(tag, rws, lane == 0 ? WS_FH + cl * 1024 : OOB, 0);
    STAMP(2);
    if (packer) {
      SB();
      if (rank < 7) {
#pragma unroll
        for (int u = 0; u < 4; ++u) {
          v4u f0, f1;
          xpose(rd + 8 * u, f0, f1);
          const int dst = WS_P2 + rank * 229376 + (pk_ * 4 + u) * 2048;
          st128c(f0, rws, l16, dst);
          st128c(f1, rws, l16 + 1024, dst);
        }
      } else {
#pragma unroll
        for (int i = 0; i < 7; ++i) {
          const float v = silu(lo16(cv[i]));
          *(LAS unsigned short*)(L + L_SC + i * 128 + lane * 2) = (unsigned short)pk(v, v);
        }
      }
    }
    STAMP(3);
    asm volatile("" ::"v"(codepf));
#pragma unroll
    for (int n = 0; n < NQW; ++n) wq_load(n);
    STAMP(4);
  } else {
    const int v = w - 1;
    if (!packer) {
      const int uq = xcd + 8 * (j >> 4), ukh = (j >> 3) & 1;
      if (uq < 14) {
        u32 rm[16];
        const int vo = (ukh * 224 + v * 32 + g * 8) * 5376 + uq * 128 + c * 4;
        const u32 cv = ld16(rc, ukh * 448 + v * 64 + lane * 2, 0);
#pragma unroll
        for (int i = 0; i < 16; ++i) rm[i] = ld32(rwm, vo + (i >> 3) * 64, (i & 7) * 5376);
        SB();
        const float sv = silu(lo16(cv));
        *(LAS unsigned short*)(L + L_SC + v * 128 + lane * 2) = (unsigned short)pk(sv, sv);
        const v4u sf = lds128(L, L_SC + v * 128 + g * 16);
#pragma unroll
        for (int hh = 0; hh < 2; ++hh) {
          v4u f0, f1;
          xpose(rm + 8 * hh, f0, f1);
          const f32x4 a0 = mfma(f0, sf, z4), a1 = mfma(f1, sf, z4);
          if (c == 0) {
            stsf4(L, L_MODP + v * 256 + hh * 128 + g * 32, a0);
            stsf4(L, L_MODP + v * 256 + hh * 128 + g * 32 + 16, a1);
          }
        }
        asm volatile("s_waitcnt lgkmcnt(0)" ::: "memory");
        *(volatile LAS u32*)(L + L_ARR + v * 4) = tag;
        if (v == 0) {
          while ((__builtin_amdgcn_ballot_w64(*(volatile LAS u32*)(L + L_ARR + (lane < 7 ? lane : 0) * 4) != tag) != 0ull) & (budget > 0)) --budget;
          asm volatile("" ::: "memory");
          float s0 = 0.f;
#pragma unroll
          for (int k = 0; k < 7; ++k) s0 += *(LAS float*)(L + L_MODP + k * 256 + (lane >> 3) * 32 + (lane & 1) * 16 + (lane & 6) * 2);
          st64c(v2u{asu(s0), tag}, rws, WS_MU + (uq * 64 + lane) * 16 + ukh * 8, 0);
        }
      }
    }
    STAMP(1);
    int xsl[12], xrow[12];
    xsl[0] = v + 7 * ((rank * 3) >> 1);
#pragma unroll
    for (int i = 1; i < 12; ++i) xsl[i] = xsl[i - 1] + 7 >= 84 ? v : xsl[i - 1] + 7;
#pragma unroll
    for (int i = 0; i < 12; ++i) xrow[i] = xsl[i] < 78 ? xsl[i] + 2 : xsl[i] & 1;
    v4u xr[12];
#pragma unroll
    for (int i = 0; i < 12; ++i) {
      int tok = t0 - 16 + xrow[i];
      tok = tok < 0 ? 0 : tok;
      xr[i] = ld128(rx, lane < 56 ? l16 : OOB, tok * 896);
    }
    if (w == 7 && packer) {
      const int q = rank * 28 + pk_;
      const u64 wa = (u64)pwo, wm = (u64)pwm;
      dma4(v4u{(u32)wa, (u32)(wa >> 32) & 0xFFFFu, (u32)(D * D * 2), 0x00020000u}, lane < 14 ? q * 1792 + lane * 128 : OOB, L_DUMMY);
      dma4(v4u{(u32)wm, (u32)(wm >> 32) & 0xFFFFu, (u32)(D * 6 * D * 2), 0x00020000u},
           lane < 56 ? (2 * q + (lane >= 28)) * 5376 + 1792 + (lane >= 28 ? lane - 28 : lane) * 128 : OOB, L_DUMMY);
    }
    SB();
    f32x2 t[12][4];
    {
      float sm[12], sq[12];
#pragma unroll
      for (int i = 0; i < 12; ++i) {
        sm[i] = sq[i] = 0.f;
#pragma unroll
        for (int e = 0; e < 4; ++e) {
          sm[i] = dot2(xr[i][e], 0x3F803F80u, sm[i]);
          sq[i] = dot2(xr[i][e], xr[i][e], sq[i]);
        }
      }
      STAMP(2);
      float rs_[3], nn[3];
#pragma unroll
      for (int q = 0; q < 3; ++q) {
        const float m = rowsum(fold16(fold32(sm[4 * q], sm[4 * q + 2]), fold32(sm[4 * q + 1], sm[4 * q + 3]))) * (1.f / D);
        const float vr = rowsum(fold16(fold32(sq[4 * q], sq[4 * q + 2]), fold32(sq[4 * q + 1], sq[4 * q + 3]))) * (1.f / D);
        rs_[q] = __builtin_amdgcn_rsqf(fmaxf(vr - m * m, 0.f) + 1e-5f);
        nn[q] = -m * rs_[q];
        int r = xsl[4 * q] + 7 * g;
        r = r >= 84 ? r - 84 : r;
        if (c == 0) *(LAS float*)(L + L_MU1 + (r + 2) * 4) = m;
      }
#pragma unroll
      for (int i = 0; i < 12; ++i) {
        const float r = asf(__builtin_amdgcn_readlane(asu(rs_[i >> 2]), 16 * (i & 3)));
        float nv = asf(__builtin_amdgcn_readlane(asu(nn[i >> 2]), 16 * (i & 3)));
        asm("" : "+v"(nv));
#pragma unroll
        for (int e = 0; e < 4; ++e) {
          t[i][e] = fma2(up2(xr[i][e]), f32x2{r, r}, f32x2{nv, nv});
          asm volatile("" : "+v"(t[i][e]));
        }
      }
    }
    STAMP(3);
    if ((lane >> 3) == rank) {
#pragma unroll
      for (int i = 0; i < 12; ++i) sts128(L, L_XR - 2048 + xrow[i] * 128 + (((lane & 7) ^ (xrow[i] & 7)) << 4), xr[i]);
    }
    SB();
    if (v < 4) {
#pragma unroll
      for (int i = 0; i < 14; i += 2) slab2(v * 14 + i);
    }
    if (v == 6 && lane < 32) sts128(L, L_B + 57344 + l16, v4u{0u, 0u, 0u, 0u});
    if (v < 4) {
      const v4u sv = {SENT, SENT, SENT, SENT};
      st128c(sv, rws, l16, o_cl + (v * 16 + 2 * rank) * 1024);
      st128c(sv, rws, l16 + 1024, o_cl + (v * 16 + 2 * rank) * 1024);
      if (rank < 7) {
        st128c(sv, rws, l16, x_cl + (v * 14 + 2 * rank) * 1024);
        st128c(sv, rws, l16 + 1024, x_cl + (v * 14 + 2 * rank) * 1024);
        st128c(sv, rws, g == 0 ? l16 : OOB, x_cl + XS_OFF + (v * 7 + rank) * 256);
      }
    }
    while ((__builtin_amdgcn_readfirstlane(*(volatile LAS u32*)(L + L_GO)) != tag) & (budget > 0)) {
      __builtin_amdgcn_s_sleep(1);
      --budget;
    }
    asm volatile("" ::: "memory");
    STAMP(4);
#pragma unroll
    for (int n = 0; n < 4; ++n) wq_load(n);
    SB();
    if (lane < 56) {
      const f32x4 ca[2] = {ldsf4(L, L_PAIR + 1792 + 2 * l16), ldsf4(L, L_PAIR + 1808 + 2 * l16)}, sa[2] = {ldsf4(L, L_PAIR + 2 * l16), ldsf4(L, L_PAIR + 16 + 2 * l16)};
      const int base = L_A + ((lane >> 2) * 64 + (lane & 3) * 16) * 16, l7 = (lane & 7) * 16;
#pragma unroll
      for (int i = 0; i < 12; ++i) {
        v4u hv;
        hv[0] = pk2(fma2(t[i][0], ca[0].xy, sa[0].xy));
        hv[1] = pk2(fma2(t[i][1], ca[0].zw, sa[0].zw));
        hv[2] = pk2(fma2(t[i][2], ca[1].xy, sa[1].xy));
        hv[3] = pk2(fma2(t[i][3], ca[1].zw, sa[1].zw));
        sts128(L, base + (l7 ^ ((xrow[i] & 15) * 16)) + (xrow[i] >> 4) * 14336, hv);
      }
    }
    SB();
#pragma unroll
    for (int n = 4; n < NQW; ++n) wq_load(n);
    if (rank == 7) waitvm<NQW>();
  }
  bar();
  STAMP(5);
  if (rank == 7 && packer && w < 4) {
    const int wb = L_B + (8 * g + (c >> 2)) * 128 + ((w ^ ((c >> 3) | ((g & 1) << 1))) << 5) + (c & 3) * 8;
    f32x4 acc = z4;
#pragma unroll
    for (int s2 = 0; s2 < 2; ++s2) {
      v4u sf[7], af[7];
#pragma unroll
      for (int q = 0; q < 7; ++q) {
        const int s = 7 * s2 + q;
        const v2u lo = trread(L, wb + s * 4096), hi = trread(L, wb + s * 4096 + 512);
        af[q] = v4u{lo[0], lo[1], hi[0], hi[1]};
        sf[q] = lds128(L, L_SC + s * 64 + g * 16);
      }
#pragma unroll
      for (int q = 0; q < 7; ++q) acc = mfma(af[q], sf[q], acc);
    }
    st128c(v4u{pk(acc[0], acc[1]), tag, pk(acc[2], acc[3]), tag}, rws, c == 0 ? WS_MOD + pk_ * 256 + w * 64 + g * 16 : OOB, 0);
  }

  v4u w1f[28];
  const int w1o = WS_P1 + rank * 200704 + w * 28672, w1v = w < 7 ? l16 : OOB;
  auto w1_load = [&](int n) LAM { w1f[n] = ld128c(rws, w1v + (n & 3) * 1024, w1o + (n >> 2) * 4096); };
  f32x4 akv[5], aq[4];
  const int fbo = w != 0 ? OOB : WS_FB + cl * 128 + (lane & 31) * 4;
  u32 fb;
  {
    const int hb0 = L_A + (g * 16 + (c ^ g)) * 16, hb1 = L_A + (g * 16 + (c ^ (4 + g))) * 16;
    const int ad[2][2] = {{(kh ? hb1 : hb0) + kh * 7168, (kh ? hb0 : hb1) + kh * 7168}, {(kh ? hb0 : hb1) + (1 - kh) * 7168, (kh ? hb1 : hb0) + (1 - kh) * 7168}};
#pragma unroll
    for (int r = 0; r < 5; ++r) akv[r] = f32x4{0.f, 0.f, 0.f, 0.f};
#pragma unroll
    for (int r = 0; r < 4; ++r) aq[r] = f32x4{0.f, 0.f, 0.f, 0.f};
    v4u hf[HFD];
    auto h_read = [&](int n) LAM { const int s = n / 5, r = n % 5; return lds128(L, ad[s & 1][(s >> 1) & 1] + r * 14336 + (s >> 1) * 1024); };
    u32 f1;
    const int f1o = lane < 14 ? WS_FA + rank * 1024 + (2 * lane + (w >> 2)) * 32 + 4 : OOB;
    const u64 f1m = w < 7 ? 0x3FFFull : 0ull;
#pragma unroll
    for (int n = 0; n < HFD; ++n) hf[n] = h_read(n);
    SB();
#pragma unroll
    for (int s = 0; s < 14; ++s) {
#pragma unroll
      for (int r = 0; r < 5; ++r) {
        akv[r] = mfma(wqf[3 * (s >> 1) + 2 * (s & 1)], hf[(5 * s + r) % HFD], akv[r]);
        if (!(s & 1) && r > 0) aq[r - 1] = mfma(wqf[3 * (s >> 1) + 1], hf[(5 * s + r) % HFD], aq[r - 1]);
        if (5 * s + r + HFD < 70) hf[(5 * s + r) % HFD] = h_read(5 * s + r + HFD);
        if (r == 0 && NQW + 3 * (s >> 1) + 2 * (s & 1) < 21) wq_load(NQW + 3 * (s >> 1) + 2 * (s & 1));
        if (r == 3 && !(s & 1) && NQW + 3 * (s >> 1) + 1 < 21) wq_load(NQW + 3 * (s >> 1) + 1);
        if (s == 0 && r == 4) st32c(tag, rws, lane != 0 || w > 4 ? OOB : w == 0 ? WS_FA + rank * 1024 + pk_ * 32 + 8 : WS_FB + cl * 128 + rank * 16 + (w - 1) * 4, 0);
        if (s == 11 && r == 0) fb = ld32c(rws, fbo, 0);
        if (s == 8 && r == 2) f1 = ld32c(rws, f1o, 0);
        if (s == 11 && r == 4) {
          while (((__builtin_amdgcn_ballot_w64(f1 != tag) & f1m) != 0ull) & (budget > 0)) {
            asm volatile("" ::: "memory");
            __builtin_amdgcn_s_sleep(PSLEEP);
      f1 = ld32c(rws, f1o, 0);
            --budget;
          }
          asm volatile("" ::: "memory");
        }
        if (s >= 12 && (r == 0 || r == 3)) w1_load(2 * (s - 12) + (r ? 1 : 0));
        SB();
      }
    }
  }
  if (w == 0) {
    while ((__builtin_amdgcn_ballot_w64(fb != tag) != 0ull) & (budget > 0)) {
      asm volatile("" ::: "memory");
      __builtin_amdgcn_s_sleep(PSLEEP);
      fb = ld32c(rws, fbo, 0);
      --budget;
    }
  }
  const u64 wsa = (u64)pws;
  const v4u rsw = {(u32)wsa, (u32)(wsa >> 32) & 0xFFFFu, rank < 7 ? (u32)WS_END : 0u, 0x00020000u};
  f32x4 bias[2];
  {
    const int tq = t0 + 16 * (w >> 1) + c;
#pragma unroll
    for (int e = 0; e < 8; ++e) {
      const int delta = c + (e < 4 ? 16 : 0) - 4 * g - (e & 3);
      bias[e >> 2][e & 3] = ((delta & ~14) == 0 && delta <= tq) ? 0.f : -1.0e30f;
    }
    asm volatile("" : "+v"(bias[0]), "+v"(bias[1]));
  }
  STAMP(6);
  bar();
  const int mmo = w < 7 ? WS_MOD + w * 1024 + l16 : WS_FA + rank * 1024 + (lane & 31) * 32;
  v4u mm = ld128c(rws, mmo, 0);
#pragma unroll
  for (int n = 4; n < 8; ++n) w1_load(n);
  SB();
  {
    const int qb = L_A + ((w >> 1) & 1) * QK_HD + l16;
#pragma unroll
    for (int r = 0; r < 5; ++r) sts64(L, qb + (kh * 5 + r) * 1024 + (w & 1) * 8, v2u{pk(akv[r][0], akv[r][1]), pk(akv[r][2], akv[r][3])});
#pragma unroll
    for (int r = 0; r < 4; ++r) stsf4(L, qb + (10 + kh * 8 + 2 * r + (w & 1)) * 1024, aq[r]);
  }
  bar();
  {
    const int rt = w >> 1, hd = w & 1;
    const int qb = L_A + hd * QK_HD + l16 + rt * 1024;
    const int qq = L_A + hd * QK_HD + l16 + (10 + 2 * rt) * 1024;
    const f32x4 qa = ldsf4(L, qq) + ldsf4(L, qq + 8192), qc = ldsf4(L, qq + 1024) + ldsf4(L, qq + 9216);
    const v4u k0 = lds128(L, qb), k1 = lds128(L, qb + 1024);
    const int vb = L_A + hd * QK_HD + (5 + rt) * 1024 + ((c & 3) * 16 + 4 * g + (c >> 2)) * 16;
    const v2u v00 = trread(L, vb), v01 = trread(L, vb + 8), v10 = trread(L, vb + 1024), v11 = trread(L, vb + 1032);
    const v4u qf = {pk(qa[0], qa[1]), pk(qa[2], qa[3]), pk(qc[0], qc[1]), pk(qc[2], qc[3])};
    const f32x4 z = {0.f, 0.f, 0.f, 0.f};
    const f32x4 s0 = mfma(k0, qf, bias[0]), s1 = mfma(k1, qf, bias[1]);
#pragma unroll
    for (int n = 8; n < 12; ++n) w1_load(n);
    SB();
    const float sc = 0.18898223650461362f * 1.44269504088896341f;
    const float mx = -sc * gmax(fmaxf(fmaxf(fmaxf(s0[0], s0[1]), fmaxf(s0[2], s0[3])), fmaxf(fmaxf(s1[0], s1[1]), fmaxf(s1[2], s1[3]))));
    float sv[8];
    float sum = 0.f;
#pragma unroll
    for (int e = 0; e < 8; ++e) {
      sv[e] = __builtin_amdgcn_exp2f(vfma(e < 4 ? s0[e & 3] : s1[e & 3], sc, mx));
      sum += sv[e];
    }
    const float inv = __builtin_amdgcn_rcpf(gsum(sum));
    v4u pf;
#pragma unroll
    for (int i = 0; i < 4; ++i) pf[i] = pk(sv[2 * i] * inv, sv[2 * i + 1] * inv);
    const f32x4 o0 = mfma(v4u{v00[0], v00[1], v10[0], v10[1]}, pf, z), o1 = mfma(v4u{v01[0], v01[1], v11[0], v11[1]}, pf, z);
    v4u of;
#pragma unroll
    for (int i = 0; i < 4; ++i) of[i] = canon(pk(o0[i], o1[i]));
    st128c(of, rws, l16, o_cl + (rt * 16 + 2 * rank + hd) * 1024);
  }
  STAMP(7);
  if (w < 7) {
    while ((__builtin_amdgcn_ballot_w64((mm[1] != tag) | (mm[3] != tag)) != 0ull) & (budget > 0)) {
      asm volatile("" ::: "memory");
      __builtin_amdgcn_s_sleep(PSLEEP);
      mm = ld128c(rws, mmo, 0);
      --budget;
    }
    const int q = w * 64 + lane;
    const float one = (q >= 224 && q < 336) ? 1.f : 0.f;
    sts64(L, L_GA + (lane & ~7) * 8 + (lane & 3) * 16 + (lane & 4) * 2 + w * 512,
          v2u{pk(one + lo16(mm[0]), one + hi16(mm[0])), pk(one + lo16(mm[2]), one + hi16(mm[2]))});
  } else {
    while ((__builtin_amdgcn_ballot_w64(mm[2] != tag) != 0ull) & (budget > 0)) {
      asm volatile("" ::: "memory");
      __builtin_amdgcn_s_sleep(PSLEEP);
      mm = ld128c(rws, mmo, 0);
      --budget;
    }
  }
  SB();
  const int rt = w & 3, kx = kh ^ (rank & 1);
  const int pw = (w + rank) & 7;
  v4u x1p = {0u, 0u, 0u, 0u};
  if (rank < 7) {
    int wb[4];
#pragma unroll
    for (int x = 0; x < 4; ++x) wb[x] = L_B + (kx * 224 + 8 * g + (c >> 2)) * 128 + ((x ^ ((c >> 3) | ((g & 1) << 1))) << 5) + (c & 3) * 8;
    auto wo_read = [&](int q) LAM {
      const int hs = q >> 2, ct = q & 3;
      const v2u lo = trread(L, wb[ct ^ (((7 * hs) >> 1) & 1) * 2] + 28 * hs * 128);
      const v2u hi = trread(L, wb[ct ^ (((7 * hs + 1) >> 1) & 1) * 2] + (28 * hs + 4) * 128);
      return v4u{lo[0], lo[1], hi[0], hi[1]};
    };
    v4u wo[16];
#pragma unroll
    for (int q = 0; q < 16; ++q) wo[q] = wo_read(q);
    bar();
    const int vo = rank * 128 + kx * 64 + g * 16;
    const v4u ga = lds128(L, L_GA + vo), cb = lds128(L, L_CB + vo);
    const float mu1 = *(LAS float*)(L + L_MU1 + (16 + 16 * rt + c) * 4);
    v4u of[8];
    const int ovo = o_cl + (rt * 16 + 8 * kx) * 1024 + l16;
    issue<8>(of, rws, ovo);
    const int xo = L_XR + (16 * rt + c) * 128 + (g & 1) * 8;
    const v2u xr0 = *(LAS v2u*)(L + xo + (((kx * 4 + (g >> 1)) ^ (c & 7)) << 4)), xr1 = *(LAS v2u*)(L + xo + (((kx * 4 + 2 + (g >> 1)) ^ (c & 7)) << 4));
    SB();
    settle<8>(of, rws, ovo, budget);
    STAMP(8);
    f32x4 acc[4];
#pragma unroll
    for (int q = 0; q < 4; ++q) acc[q] = f32x4{0.f, 0.f, 0.f, 0.f};
#pragma unroll
    for (int q = 0; q < 32; ++q) {
      acc[q & 3] = mfma(wo[q & 15], of[q >> 2], acc[q & 3]);
      if (q < 16) wo[q] = wo_read(16 + q);
      if (q % 2 == 1 && 12 + q / 2 < N1C) w1_load(12 + q / 2);
      SB();
    }
#pragma unroll
    for (int q = 0; q < 4; ++q) stsf4(L, L_A + ((kx * 4 + rt) * 4 + q) * 1024 + l16, acc[q]);
    bar();
    const int pb = L_A + (rt * 4 + 2 * kx) * 1024 + l16;
    const f32x4 a0 = ldsf4(L, pb) + ldsf4(L, pb + 16384), a1 = ldsf4(L, pb + 1024) + ldsf4(L, pb + 17408);
    const v4u xres = {xr0[0], xr0[1], xr1[0], xr1[1]};
    v4u xp;
    f32x2 s1v = {0.f, 0.f}, s2v = {0.f, 0.f};
#pragma unroll
    for (int i = 0; i < 4; ++i) {
      x1p[i] = pk2(fma2(up2(ga[i]), i == 0 ? a0.xy : i == 1 ? a0.zw : i == 2 ? a1.xy : a1.zw, up2(xres[i])));
      const f32x2 d = up2(x1p[i]) - f32x2{mu1, mu1};
      s1v += d;
      s2v = fma2(d, d, s2v);
      xp[i] = canon(pk2(d * up2(cb[i])));
    }
    float s1 = s1v.x + s1v.y, s2 = s2v.x + s2v.y;
    st128c(xp, rws, l16, x_cl + (rt * 14 + 2 * rank + kx) * 1024);
    s1 = gsum(s1);
    s2 = gsum(s2);
    st64c(v2u{canon(asu(s1)), canon(asu(s2))}, rws, g == 0 ? l16 : OOB, x_cl + XS_OFF + (rt * 7 + rank) * 256 + kx * 8);
  } else {
    bar();
#pragma unroll
    for (int n = 12; n < N1C; ++n) w1_load(n);
    const v4u sv = {SENT, SENT, SENT, SENT};
#pragma unroll 2
    for (int i = 0; i < 14; ++i) st128c(sv, rws, l16 + i * 1024, h_cl + w * 28672 + 7168);
    drain();
    bar();
    if (tid == 0) st32c(tag, rws, WS_FH + cl * 1024 + 4, 0);
  }
  STAMP(9);
  v4u w2f[28];
  f32x4 u0 = z4, u1 = z4;
  const int w2o = WS_P2 + rank * 229376 + pw * 28672, w2v = rank < 7 ? l16 : OOB;
  auto w2_load = [&](int n) LAM { w2f[n] = ld128c(rws, w2v + (n & 3) * 1024, w2o + (n >> 2) * 4096); };
  constexpr int L_W2 = 57344, NST = 28 - N2B;
  static_assert(L_W2 + 8 * NST * 1024 <= L_GA, "W2 staging");
  const v4u rsh = {(u32)wsa, (u32)(wsa >> 32) & 0xFFFFu, (u32)WS_END, 0x00020000u};
  {
    v4u xf[7], xs[1];
    const int pr = 4 * kx + g;
    const int xo = x_cl + (rt * 14 + 7 * kx) * 1024 + l16, so = pr < 7 ? x_cl + XS_OFF + (rt * 7 + pr) * 256 + c * 16 : OOB;
    const int vb = ((c & 1) ? L_SB : L_CB) + g * 16;
    {
      v4u bfv[N1C / 2];
#pragma unroll
      for (int s = 0; s < N1C / 2; ++s) bfv[s] = lds128(L, vb + s * 64);
      SB();
#pragma unroll
      for (int s = 0; s < N1C / 2; ++s) {
        u0 = mfma(w1f[2 * s], bfv[s], u0);
        u1 = mfma(w1f[2 * s + 1], bfv[s], u1);
      }
    }
    bar();
    issue<7>(xf, rws, xo);
    issue<1>(xs, rws, so);
    const int hro = w == 0 ? WS_FH + cl * 1024 + (lane & 1) * 4 : OOB;
    u32 hr = ld32c(rws, hro, 0);
    SB();
    settle<7>(xf, rws, xo, budget);
    settle<1>(xs, rws, so, budget);
    if (w == 0) {
      while ((__builtin_amdgcn_ballot_w64(hr != tag) != 0ull) & (budget > 0)) {
        asm volatile("" ::: "memory");
        __builtin_amdgcn_s_sleep(PSLEEP);
        hr = ld32c(rws, hro, 0);
        --budget;
      }
    }
#pragma unroll
    for (int n = N1C; n < 28; ++n) w1_load(n);
    SB();
#pragma unroll
    for (int i = 0; i < 7; ++i) sts128(L, L_A + (rt * 14 + 7 * kx + i) * 1024 + l16, xf[i]);
    const float s1 = gsum(asf(xs[0][0]) + asf(xs[0][2])), s2 = gsum(asf(xs[0][1]) + asf(xs[0][3]));
    if (g == 0) sts64(L, L_ST + ((rt * 2 + kx) * 16 + c) * 8, v2u{asu(s1), asu(s2)});
    SB();
#pragma unroll
    for (int n = 0; n < 4; ++n) w2_load(n);
  }
  STAMP(10);
  bar();
  STAMP(11);
  v4u hf[4][7];
  const int ho = rank < 7 ? h_cl + pw * 28672 + l16 : OOB;
  if (w < 7) {
    v4u xf[4];
#pragma unroll
    for (int i = 0; i < 4; ++i) xf[i] = lds128(L, L_A + i * 1024 + l16);
    const int ub = L_UV + w * 256 + g * 64;
    const int vb = ((c & 1) ? L_SB : L_CB) + g * 16;
    f32x4 uu[2], vv[2];
    SB();
    const float nl2e = -1.44269504f;
#pragma unroll
    for (int r = 0; r < 4; ++r) {
      f32x4 a[2] = {z4, z4};
      v2u sta, stb;
#pragma unroll
      for (int s = 0; s < 14; ++s) {
        const int n = r * 14 + s;
        a[0] = mfma(w1f[2 * s], xf[n & 3], a[0]);
        a[1] = mfma(w1f[2 * s + 1], xf[n & 3], a[1]);
        if (n + 4 < 56) xf[n & 3] = lds128(L, L_A + (n + 4) * 1024 + l16);
        if (s == 8) {
          sta = *(LAS v2u*)(L + L_ST + (r * 32 + c) * 8);
          stb = *(LAS v2u*)(L + L_ST + (r * 32 + 16 + c) * 8);
        }
        if (r == 0 && s >= N1C / 2) {
          const v4u bv = lds128(L, vb + s * 64);
          u0 = mfma(w1f[2 * s], bv, u0);
          u1 = mfma(w1f[2 * s + 1], bv, u1);
        }
        if (n >= 4 && n % 2 == 0 && 4 + (n - 4) / 2 < N2B) w2_load(4 + (n - 4) / 2);
        if (r == 3 && s >= 7) hf[0][s - 7] = ld128c(rws, ho + ((s - 7) & 3) * 1024, ((s - 7) >> 2) * 4096);
        SB();
      }
      if (r == 0) {
        const float sc = (c & 1) ? nl2e : 1.f;
        if (c < 2) {
          stsf4(L, ub + c * 32, u0 * sc);
          stsf4(L, ub + c * 32 + 16, u1 * sc);
        }
        uu[0] = ldsf4(L, ub);
        uu[1] = ldsf4(L, ub + 16);
        vv[0] = ldsf4(L, ub + 32);
        vv[1] = ldsf4(L, ub + 48);
      }
      const float mu = (asf(sta[0]) + asf(stb[0])) * (1.f / D);
      const float var = fmaxf((asf(sta[1]) + asf(stb[1])) * (1.f / D) - mu * mu, 0.f);
      const float rsl = nl2e * __builtin_amdgcn_rsqf(var + 1e-5f);
      float y[8];
#pragma unroll
      for (int e = 0; e < 8; ++e) {
        const float z = vfma(vfma(-mu, uu[e & 1][e >> 1], a[e & 1][e >> 1]), rsl, vv[e & 1][e >> 1]);
        y[e] = z * __builtin_amdgcn_rcpf(vfma(__builtin_amdgcn_exp2f(z), nl2e, nl2e));
      }
      const v4u hv = {canon(pkn(y[0], y[1])), canon(pkn(y[2], y[3])), canon(pkn(y[4], y[5])), canon(pkn(y[6], y[7]))};
      if (r == 3) {
        while ((__builtin_amdgcn_readfirstlane(*(volatile LAS u32*)(L + L_GO + 8)) != tag) & (budget > 0)) {
          __builtin_amdgcn_s_sleep(1);
          --budget;
        }
        asm volatile("" ::: "memory");
      }
      st128c(hv, rws, l16, h_cl + (rank * 28 + w + r * 7) * 1024);
      SB();
    }
  } else {
    if (rank == 7) {
      const v4u sv = {SENT, SENT, SENT, SENT};
#pragma unroll 1
      for (int p = 0; p < 8; ++p) {
#pragma unroll
        for (int i = 0; i < 7; ++i) st128c(sv, rws, l16 + i * 1024, h_cl + p * 28672 + 21504);
      }
      drain();
      st32c(tag, rws, lane == 0 ? WS_FH + cl * 1024 + 8 : OOB, 0);
    }
#pragma unroll
    for (int n = 4; n < N2B; ++n) w2_load(n);
#pragma unroll 1
    for (int ww = 0; ww < 8; ++ww) {
#pragma unroll
      for (int q = 0; q < NST; ++q) dma1k(rsw, l16, WS_P2 + rank * 229376 + (ww * 28 + N2B + q) * 1024, L_W2 + (ww * NST + q) * 1024);
      if (ww == 4 || ww == 7) {
        bool ok;
        do {
          asm volatile("" ::: "memory");
          ok = __builtin_amdgcn_readfirstlane(ld32c(rws, WS_FH + cl * 1024 + 8, 0)) == tag;
          --budget;
        } while (!ok & (ww == 7) & (budget > 0));
        if (ok | (ww == 7)) *(volatile LAS u32*)(L + L_GO + 8) = tag;
      }
    }
    drain();
    issue<7>(hf[0], rws, ho);
  }
  STAMP(12);
  if (rank < 7) {
    bar();
    issue<7>(hf[1], rws, ho + 7168);
#pragma unroll
    for (int n = 0; n < NST; ++n) w2f[N2B + n] = lds128(L, L_W2 + (pw * NST + n) * 1024 + l16);
    SB();
#pragma unroll
    for (int r = 0; r < 4; ++r) {
      settle<7>(hf[r], rws, ho + r * 7168, budget);
      if (r < 2) issue<7>(hf[r + 2], rws, ho + (r + 2) * 7168);
      SB();
      f32x4 acc[4];
#pragma unroll
      for (int q = 0; q < 4; ++q) acc[q] = z4;
#pragma unroll
      for (int s = 0; s < 7; ++s) {
#pragma unroll
        for (int q = 0; q < 4; ++q) acc[q] = mfma(w2f[4 * s + q], hf[r][s], acc[q]);
      }
      SB();
      if (r == 1) bar();
#pragma unroll
      for (int q = 0; q < 4; ++q) stsf4(L, ((r * 8 + w) * 4 + q) * 1024 + l16, acc[q]);
      SB();
    }
    STAMP(13);
    const v4u gb = lds128(L, L_GB + rank * 128 + kx * 64 + g * 16);
    bar();
    f32x4 pa[8][2];
#pragma unroll
    for (int p = 0; p < 8; ++p) {
      pa[p][0] = ldsf4(L, ((rt * 8 + p) * 4 + 2 * kx) * 1024 + l16);
      pa[p][1] = ldsf4(L, ((rt * 8 + p) * 4 + 2 * kx + 1) * 1024 + l16);
    }
    SB();
    f32x4 a0 = pa[0][0], a1 = pa[0][1];
#pragma unroll
    for (int p = 1; p < 8; ++p) {
      a0 += pa[p][0];
      a1 += pa[p][1];
    }
    v4u ov;
#pragma unroll
    for (int i = 0; i < 4; ++i) {
      ov[i] = pkn(vfma(lo16(gb[i]), a0[i], lo16(x1p[i])), vfma(hi16(gb[i]), a1[i], hi16(x1p[i])));
    }
    const rsrc_t rout = mkrs(pout, S * D * 2);
    const auto e0 = __builtin_amdgcn_permlane16_swap(ov[0], ov[2], false, false), e1 = __builtin_amdgcn_permlane16_swap(ov[1], ov[3], false, false);
    const int oo = c * 896 + (g & 1) * 32 + (g >> 1) * 16 + (t0 + 16 * rt) * 896 + rank * 128 + kx * 64;
    __builtin_amdgcn_raw_buffer_store_b128(v4u{e0[0], e1[0], e0[1], e1[1]}, rout, oo, 0, 0);
  } else if (cl == 0 && w == 7) {
    for (;;) {
      asm volatile("" ::: "memory");
      bool ok = true;
#pragma unroll
      for (int i = 0; i < 4; ++i) {
        const v4u f = ld128c(rws, l16 + i * 1024, WS_FB);
        ok = ok & (f[0] == tag) & (f[1] == tag) & (f[2] == tag) & (f[3] == tag);
      }
      --budget;
      if ((__builtin_amdgcn_ballot_w64(!ok) == 0ull) | (budget <= 0)) break;
      __builtin_amdgcn_s_sleep(8);
    }
    if (lane == 0) st32c(tag, rws, WS_EPOCH, 0);
  }
  STAMP(14);
  asm volatile(".Lfk_pc1:");
}
}

extern "C" void kernel_launch(void* const* d_in, const int* in_sizes, int n_in, void* d_out, int out_size, void* d_ws,
                              size_t ws_size, hipStream_t stream) {
  (void)hipFuncSetAttribute((const void*)fk::fused, hipFuncAttributeMaxDynamicSharedMemorySize, fk::L_END);
  fk::fused<<<256, 512, fk::L_END, stream>>>(d_in[0], d_in[1], d_in[2], d_in[4], d_in[5], d_in[6], d_in[7], d_out, d_ws);
}
```

```cpp
#include <hip/hip_runtime.h>

typedef unsigned u32;
typedef unsigned long long u64;
typedef u32 v4u __attribute__((ext_vector_type(4)));
typedef u32 v2u __attribute__((ext_vector_type(2)));
typedef float f32x4 __attribute__((ext_vector_type(4)));
typedef float f32x2 __attribute__((ext_vector_type(2)));
typedef short bf16x8 __attribute__((ext_vector_type(8)));
typedef __bf16 bf16x2 __attribute__((ext_vector_type(2)));
typedef __amdgpu_buffer_rsrc_t rsrc_t;
#define LAS __attribute__((address_space(3)))
#define SB() __builtin_amdgcn_sched_barrier(0)
#define NOPK
#define DEV __device__ __forceinline__ NOPK
#define LAM __attribute__((always_inline)) NOPK
#define fmaxf __builtin_fmaxf

namespace fk {
constexpr int S = 2048, D = 448, F = 1792;
constexpr int WS_EPOCH = 0;
constexpr int WS_FA = 81920;
constexpr int WS_FB = 8192;
constexpr int WS_FH = 32768;
constexpr int WS_MOD = 16384;
constexpr int WS_MU = 65536;
constexpr int WS_O = 131072;
constexpr int WS_X = WS_O + 32 * 65536;
constexpr int XS_OFF = 57344;
constexpr int WS_H = WS_X + 32 * 65536;
constexpr int H_CL = 8 * 4 * 7 * 1024;
constexpr int WS_PQ = WS_H + 32 * H_CL;
constexpr int WS_PO = WS_PQ + 8 * 168 * 1024;
constexpr int WS_P2 = WS_PO + 8 * 65536;
constexpr int WS_P1 = WS_P2 + 8 * 224 * 1024;
constexpr int WS_END = WS_P1 + 8 * 196 * 1024;
constexpr int L_A = 0;
constexpr int L_B = 71680;
constexpr int L_PAIR = 137216;
constexpr int L_MODP = 141312;
constexpr int L_SC = 143360;
constexpr int L_XR = 146432;
constexpr int L_MU1 = 154688;
constexpr int L_GA = 155648, L_SB = L_GA + 896, L_CB = L_SB + 896, L_GB = L_CB + 896;
constexpr int L_ST = L_GB + 896;
constexpr int L_UV = L_ST + 1024;
constexpr int L_CNT = L_UV + 1792;
constexpr int L_GO = L_CNT + 16, L_ARR = L_CNT + 32;
constexpr int L_DUMMY = L_CNT + 64;
constexpr int L_END = L_DUMMY + 1024;
static_assert(L_END <= 163840, "LDS");
constexpr int QK_HD = 26 * 1024;
constexpr u32 SENT = 0xFFFFFFFFu;
constexpr int SPIN_CAP = 1 << 16;
constexpr int OOB = 0x40000000;
constexpr int CODE_SLICES = 8;
#define PSLEEP 6
#define NPF 14
#define HFD 8
#define NQW 4
#define N1B 12
#define N1C 22
#define N2A 8
#define N2B 16

DEV float asf(u32 x) { return __builtin_bit_cast(float, x); }
DEV u32 asu(float x) { return __builtin_bit_cast(u32, x); }
DEV float lo16(u32 x) { return asf(x << 16); }
DEV float hi16(u32 x) { return asf(x & 0xFFFF0000u); }
DEV u32 pk(float a, float b) {
  f32x2 v = {a, b};
  return __builtin_bit_cast(u32, __builtin_convertvector(v, bf16x2));
}
DEV u32 pk2(f32x2 v) { return __builtin_bit_cast(u32, __builtin_convertvector(v, bf16x2)); }
DEV u32 pkn(float a, float b) {
  asm("" : "+v"(a));
  asm("" : "+v"(b));
  return pk(a, b);
}
DEV f32x2 up2(u32 x) { return f32x2{lo16(x), hi16(x)}; }
DEV f32x2 fma2(f32x2 a, f32x2 b, f32x2 c) { return __builtin_elementwise_fma(a, b, c); }
DEV u32 canon(u32 v) { return v < 0xFFFFFFFEu ? v : 0xFFFFFFFEu; }
DEV f32x4 mfma(v4u a, v4u b, f32x4 c) {
  return __builtin_amdgcn_mfma_f32_16x16x32_bf16(__builtin_bit_cast(bf16x8, a), __builtin_bit_cast(bf16x8, b), c, 0, 0, 0);
}
DEV float dot2(u32 a, u32 b, float c) {
  return __builtin_amdgcn_fdot2_f32_bf16(__builtin_bit_cast(bf16x2, a), __builtin_bit_cast(bf16x2, b), c, false);
}
DEV rsrc_t mkrs(const void* p, int bytes) { return __builtin_amdgcn_make_buffer_rsrc((void*)p, 0, bytes, 0x00020000); }
DEV u32 ld32(rsrc_t r, int v, int s) { return __builtin_amdgcn_raw_buffer_load_b32(r, v, s, 0); }
DEV v4u ld128(rsrc_t r, int v, int s) { return __builtin_amdgcn_raw_buffer_load_b128(r, v, s, 0); }
DEV u32 ld16(rsrc_t r, int v, int s) { return __builtin_amdgcn_raw_buffer_load_b16(r, v, s, 0); }
DEV v2u ld64(rsrc_t r, int v, int s) { return __builtin_amdgcn_raw_buffer_load_b64(r, v, s, 0); }
DEV v2u ld64c(rsrc_t r, int v, int s) { return __builtin_amdgcn_raw_buffer_load_b64(r, v, s, 16); }
DEV u32 ld32c(rsrc_t r, int v, int s) { return __builtin_amdgcn_raw_buffer_load_b32(r, v, s, 16); }
DEV v4u ld128c(rsrc_t r, int v, int s) { return __builtin_amdgcn_raw_buffer_load_b128(r, v, s, 16); }
DEV void st128c(v4u x, rsrc_t r, int v, int s) { __builtin_amdgcn_raw_buffer_store_b128(x, r, v + s, 0, 16); }
DEV void st64c(v2u x, rsrc_t r, int v, int s) { __builtin_amdgcn_raw_buffer_store_b64(x, r, v, s, 16); }
DEV void st32c(u32 x, rsrc_t r, int v, int s) { __builtin_amdgcn_raw_buffer_store_b32(x, r, v, s, 16); }
DEV v4u lds128(LAS char* L, int off) { return *(LAS v4u*)(L + off); }
DEV f32x4 ldsf4(LAS char* L, int off) { return *(LAS f32x4*)(L + off); }
DEV void sts128(LAS char* L, int off, v4u x) { *(LAS v4u*)(L + off) = x; }
DEV void stsf4(LAS char* L, int off, f32x4 x) { *(LAS f32x4*)(L + off) = x; }
DEV void sts64(LAS char* L, int off, v2u x) { *(LAS v2u*)(L + off) = x; }
typedef short v4s __attribute__((ext_vector_type(4)));
DEV v2u trread(LAS char* L, int off) { return __builtin_bit_cast(v2u, __builtin_amdgcn_ds_read_tr16_b64_v4i16((LAS v4s*)(L + off))); }
DEV void bar() {
  asm volatile("s_waitcnt lgkmcnt(0)" ::: "memory");
  __builtin_amdgcn_s_barrier();
  asm volatile("" ::: "memory");
}
DEV void drain() { asm volatile("s_waitcnt vmcnt(0)" ::: "memory"); }
DEV void drain_seen() { __builtin_amdgcn_s_waitcnt(0x0F70); asm volatile("" ::: "memory"); }
template <int N>
DEV void waitvm() { asm volatile("s_waitcnt vmcnt(%0)" ::"n"(N) : "memory"); }
DEV void pin(v4u& x) { asm volatile("" : "+v"(x)); }
DEV void dma1k(v4u rs, int voff, int soff, int lds) {
  asm volatile("s_mov_b32 m0, %2\n\ts_nop 0\n\tbuffer_load_dwordx4 %0, %1, %3 offen sc1 lds" ::"v"(voff), "s"(rs), "s"(lds), "s"(soff) : "memory");
}
DEV void dma1kp(v4u rs, int voff, int soff, int lds) {
  asm volatile("s_mov_b32 m0, %2\n\ts_nop 0\n\tbuffer_load_dwordx4 %0, %1, %3 offen lds" ::"v"(voff), "s"(rs), "s"(lds), "s"(soff) : "memory");
}
DEV void dma4(v4u rs, int voff, int lds) {
  asm volatile("s_mov_b32 m0, %2\n\ts_nop 0\n\tbuffer_load_dword %0, %1, 0 offen lds" ::"v"(voff), "s"(rs), "s"(lds) : "memory");
}
DEV void xpose(const u32* r, v4u& f0, v4u& f1) {
#pragma unroll
  for (int i = 0; i < 4; ++i) {
    f0[i] = __builtin_amdgcn_perm(r[2 * i + 1], r[2 * i], 0x05040100u);
    f1[i] = __builtin_amdgcn_perm(r[2 * i + 1], r[2 * i], 0x07060302u);
  }
}
template <int CTRL>
DEV float dppadd(float x) {
  return x + asf(__builtin_amdgcn_update_dpp(0, asu(x), CTRL, 0xF, 0xF, true));
}
DEV float rowsum(float x) {
  x = dppadd<0x128>(x);
  x = dppadd<0x124>(x);
  x = dppadd<0x122>(x);
  return dppadd<0x121>(x);
}
DEV float fold32(float a, float b) {
  auto r = __builtin_amdgcn_permlane32_swap(asu(a), asu(b), false, false);
  return asf(r[0]) + asf(r[1]);
}
DEV float fold16(float a, float b) {
  auto r = __builtin_amdgcn_permlane16_swap(asu(a), asu(b), false, false);
  return asf(r[0]) + asf(r[1]);
}
DEV float gsum(float x) { x = fold16(x, x); return fold32(x, x); }
DEV float gmax(float x) {
  auto r = __builtin_amdgcn_permlane16_swap(asu(x), asu(x), false, false);
  x = fmaxf(asf(r[0]), asf(r[1]));
  auto q = __builtin_amdgcn_permlane32_swap(asu(x), asu(x), false, false);
  return fmaxf(asf(q[0]), asf(q[1]));
}
DEV u32 umax(u32 a, u32 b) { return a > b ? a : b; }
DEV u32 max4(v4u x, u32 m) {
  m = umax(umax(x[0], x[1]), m);
  return umax(umax(x[2], x[3]), m);
}
template <int N>
DEV void issue(v4u (&f)[N], rsrc_t rs, int voff) {
#pragma unroll
  for (int i = 0; i < N; ++i) f[i] = ld128c(rs, voff + (i & 3) * 1024, (i >> 2) * 4096);
}
template <int N>
DEV bool pending(const v4u (&f)[N]) {
  u32 m = 0;
#pragma unroll
  for (int i = 0; i < N; ++i) m = max4(f[i], m);
  return __builtin_amdgcn_ballot_w64(m == SENT) != 0ull;
}
DEV bool pend1(v4u f) { return __builtin_amdgcn_ballot_w64(max4(f, 0u) == SENT) != 0ull; }
template <int N>
DEV void settle(v4u (&f)[N], rsrc_t rs, int voff, int& budget) {
  if (pending<N>(f)) {
    bool again;
    do {
      __builtin_amdgcn_s_sleep(PSLEEP);
      asm volatile("" ::: "memory");
#pragma unroll
      for (int i = 0; i < N; ++i)
        if (pend1(f[i])) f[i] = ld128c(rs, voff + (i & 3) * 1024, (i >> 2) * 4096);
      --budget;
      again = pending<N>(f) & (budget > 0);
    } while (again);
  }
}
DEV float vfma(float a, float b, float c) { return __builtin_fmaf(a, b, c); }
DEV float vfma_s(float a, float sb, float c) { return __builtin_fmaf(a, sb, c); }
DEV float vmul(float a, float b) { return a * b; }
DEV bool all4(v4u f, u32 tag) { return __builtin_amdgcn_ballot_w64(!((f[0] == tag) & (f[1] == tag) & (f[2] == tag) & (f[3] == tag))) == 0ull; }
DEV void wait_flags(v4u f, rsrc_t rs, int voff, u32 tag, int& budget) {
  while (!all4(f, tag) & (budget > 0)) {
    __builtin_amdgcn_s_sleep(1);
    asm volatile("" ::: "memory");
    f = ld128c(rs, voff, 0);
    --budget;
  }
  asm volatile("" ::: "memory");
}
DEV void lds_wait(LAS char* L, int off, u32 val, int& budget) {
  if (__builtin_amdgcn_readfirstlane(*(volatile LAS u32*)(L + off)) != val) {
    bool bad;
    do {
      __builtin_amdgcn_s_sleep(1);
      bad = __builtin_amdgcn_readfirstlane(*(volatile LAS u32*)(L + off)) != val;
      --budget;
    } while (bad & (budget > 0));
  }
  asm volatile("" ::: "memory");
}
DEV float silu(float x) { return x * __builtin_amdgcn_rcpf(1.f + __builtin_amdgcn_exp2f(-1.44269504f * x)); }

#define STAMP(k) do { } while (0)

__global__ void __launch_bounds__(512, 2) NOPK
fused(const void* __restrict__ px, const void* __restrict__ pwqkv, const void* __restrict__ pwo, const void* __restrict__ pw1,
      const void* __restrict__ pw2, const void* __restrict__ pc, const void* __restrict__ pwm, void* pout, void* pws) {
  extern __shared__ __attribute__((aligned(16))) char smem[];
  LAS char* L = (LAS char*)smem;
  STAMP(0);
  const int tid = threadIdx.x, lane = tid & 63, w = __builtin_amdgcn_readfirstlane(tid >> 6);
  const int c = lane & 15, g = lane >> 4, l16 = lane * 16;
  const int b = blockIdx.x, xcd = b & 7, j = b >> 3, cl = xcd * 4 + (j >> 3), rank = j & 7, t0 = cl * 64;
  int budget = SPIN_CAP;

  if (w == 7) {
    u64 pcv;
    u32 clen;
    asm volatile("s_getpc_b64 %0\n.Lfk_pc0:\n\ts_mov_b32 %1, .Lfk_pc1-.Lfk_pc0" : "=s"(pcv), "=s"(clen));
    dma4(v4u{(u32)pcv, (u32)(pcv >> 32) & 0xFFFFu, clen, 0x00020000u}, lane * 128 + (j & (CODE_SLICES - 1)) * 8192, L_DUMMY);
  }
  asm volatile("" ::"s"(px), "s"(pwqkv), "s"(pwo), "s"(pw1), "s"(pw2), "s"(pc), "s"(pwm), "s"(pout), "s"(pws));
  const rsrc_t rws = mkrs(pws, WS_END);
  const u32 tag = *(const __attribute__((address_space(4))) u32*)((const char*)pws + WS_EPOCH) + 1u;
  const int rr = (rank + xcd) & 7, pk_ = 4 * rr + (j >> 3);
  const bool packer = rr < 7;
  const f32x4 z4 = {0.f, 0.f, 0.f, 0.f};
  const rsrc_t rc = mkrs(pc, D * 2), rwm = mkrs(pwm, D * 6 * D * 2), rx = mkrs(px, S * D * 2);
  const int o_cl = WS_O + cl * 65536, x_cl = WS_X + cl * 65536, h_cl = WS_H + cl * H_CL;
  const int kh = w >> 2;
  v4u wqf[21];
  const int pe = WS_PQ + ((rank * 12 + 4 + w) * 14 + 7 * kh) * 1024, po = WS_PQ + ((rank * 12 + 4 + w) * 14 + 7 - 7 * kh) * 1024;
  const int pq = WS_PQ + ((rank * 12 + (w & 3)) * 14 + 7 * kh) * 1024;
  auto wq_load = [&](int n) LAM {
    const int i = n / 3, k = n - 3 * i;
    wqf[n] = ld128c(rws, l16 + (i & 3) * 1024, (k == 0 ? pe : k == 1 ? pq : po) + (i >> 2) * 4096);
  };
  int ad[2][2];
  f32x4 bias[2];
  auto consts = [&]() LAM {
    const int hb0 = L_A + (g * 16 + (c ^ g)) * 16, hb1 = L_A + (g * 16 + (c ^ (4 + g))) * 16;
    ad[0][0] = (kh ? hb1 : hb0) + kh * 7168;
    ad[0][1] = (kh ? hb0 : hb1) + kh * 7168;
    ad[1][0] = (kh ? hb0 : hb1) + (1 - kh) * 7168;
    ad[1][1] = (kh ? hb1 : hb0) + (1 - kh) * 7168;
    const int tq = t0 + 16 * (w >> 1) + c;
#pragma unroll
    for (int e = 0; e < 8; ++e) {
      const int delta = c + (e < 4 ? 16 : 0) - 4 * g - (e & 3);
      bias[e >> 2][e & 3] = ((delta & ~14) == 0 && delta <= tq) ? 0.f : -1.0e30f;
    }
    asm volatile("" : "+v"(bias[0]), "+v"(bias[1]), "+v"(ad[0][0]), "+v"(ad[0][1]), "+v"(ad[1][0]), "+v"(ad[1][1]));
  };
  auto slab2 = [&](int i) LAM {
    const u64 wa = rank < 7 ? (u64)pwo : (u64)pwm;
    const int st = rank < 7 ? 896 : 5376;
    const v4u rso = {(u32)wa, (u32)(wa >> 32) & 0xFFFFu, rank < 7 ? (u32)(D * D * 2) : packer ? (u32)(D * 6 * D * 2) : 0u, 0x00020000u};
    const int r8 = lane >> 3, ch = lane & 7;
    const int v0 = r8 * st + (rank < 7 ? rank * 128 : 1792 + pk_ * 128) + ((ch ^ (r8 & 2)) << 4), v1 = v0 ^ 64;
    dma1kp(rso, v0, i * 8 * st, L_B + i * 1024);
    dma1kp(rso, v1, (i + 1) * 8 * st, L_B + (i + 1) * 1024);
  };
  if (w == 0) {
    const int ks = pk_ >> 1;
    const int fa_own = lane == 0 ? WS_FA + rank * 1024 + pk_ * 32 : OOB;
    u32 rb[32];
    if (packer) {
      u32 ra[24];
      const rsrc_t rq = mkrs(pwqkv, D * 3 * D * 2), r1 = mkrs(pw1, D * F * 2);
      const int vo = c < 14 ? g * 8 * 2688 + c * 4 : OOB;
#pragma unroll
      for (int u = 0; u < 3; ++u) {
        const int pr = 3 * (pk_ & 1) + u, so = ks * 32 * 2688 + (pr >> 1) * 896 + (pr & 1) * 56 + rank * 112;
#pragma unroll
        for (int jj = 0; jj < 8; ++jj) ra[8 * u + jj] = ld32(rq, vo + (jj & 1) * 2688, so + (jj >> 1) * 5376);
      }
      SB();
#pragma unroll
      for (int u = 0; u < 3; ++u) {
        v4u f0, f1;
        xpose(ra + 8 * u, f0, f1);
        const int dst = WS_PQ + ((rank * 12 + (3 * (pk_ & 1) + u) * 2) * 14 + ks) * 1024;
        st128c(f0, rws, l16, dst);
        st128c(f1, rws, l16, dst + 14336);
      }
      SB();
      const int v1 = g * 4 * 3584 + c * 4, v1l = (pk_ & 1) ? OOB : v1, s1 = ks * 32 * 3584 + rank * 448 + (pk_ & 1) * 256;
#pragma unroll
      for (int i = 0; i < 32; ++i) rb[i] = ld32(r1, (i < 24 ? v1 : v1l) + (i & 1) * 3584 + (i >> 3) * 64, s1 + ((i & 2) + ((i >> 2) & 1) * 16) * 3584);
      waitvm<32>();
    }
    st32c(tag, rws, fa_own, 0);
    STAMP(1);
    if (!packer) {
      __builtin_amdgcn_s_sleep(64);
      const v4u sv = {SENT, SENT, SENT, SENT};
#pragma unroll 1
      for (int p = 0; p < 8; ++p) {
#pragma unroll
        for (int i = 0; i < 7; ++i) st128c(sv, rws, l16 + i * 1024, h_cl + p * 28672);
      }
    }
    u32 rd[32];
    if (packer) {
#pragma unroll
      for (int u = 0; u < 4; ++u) {
        v4u f0, f1;
        xpose(rb + 8 * u, f0, f1);
        const int dst = WS_P1 + rank * 200704 + ((4 * (pk_ & 1) + u) * 14 + ks) * 2048, vo1 = (u == 3 && (pk_ & 1)) ? OOB : l16;
        st128c(f0, rws, vo1, dst);
        st128c(f1, rws, vo1 + 1024, dst);
      }
    }
    SB();
    u32 cv[4];
#pragma unroll
    for (int i = 0; i < 4; ++i) cv[i] = ld32(rc, rank == 7 ? lane * 4 + i * 256 : OOB, 0);
    {
      v4u m[14];
      u32 fa;
#pragma unroll
      for (int i = 0; i < 14; ++i) m[i] = ld128c(rws, l16 + (i & 3) * 1024, WS_MU + (i >> 2) * 4096);
      fa = ld32c(rws, WS_FA + rank * 1024 + (lane & 31) * 32, 0);
      SB();
      *(volatile LAS u32*)(L + L_GO + 12) = tag;
      {
      SB();
      const rsrc_t r2 = mkrs(pw2, packer && rank < 7 ? F * D * 2 : 0);
      const int vo = g * 8 * 896 + rank * 128 + (c >> 2) * 8 + (c & 1) * 4 + (c & 2) * 16, so = pk_ * 64 * 896;
#pragma unroll
      for (int i = 0; i < 32; ++i) rd[i] = ld32(r2, vo + (i & 3) * 896 + ((i >> 3) & 1) * 64, so + ((i >> 4) * 32 + (i & 4)) * 896);
      }
      SB();
      bool ok = true;
#pragma unroll
      for (int i = 0; i < 14; ++i) ok = ok & (m[i][1] == tag) & (m[i][3] == tag);
      if (__builtin_amdgcn_ballot_w64(!ok) != 0ull) {
        bool bad;
        do {
          asm volatile("" ::: "memory");
#pragma unroll
          for (int i = 0; i < 14; ++i) m[i] = ld128c(rws, l16 + (i & 3) * 1024, WS_MU + (i >> 2) * 4096);
          ok = true;
#pragma unroll
          for (int i = 0; i < 14; ++i) ok = ok & (m[i][1] == tag) & (m[i][3] == tag);
          --budget;
          bad = __builtin_amdgcn_ballot_w64(!ok) != 0ull;
        } while (bad & (budget > 0));
      }
#pragma unroll
      for (int i = 0; i < 14; i += 2) {
        u32 p = pk(asf(m[i][0]) + asf(m[i][2]), asf(m[i + 1][0]) + asf(m[i + 1][2]));
        if (i >= 6) p = pk((i > 6 ? 1.f : 0.f) + lo16(p), 1.f + hi16(p));
        *(LAS float*)(L + L_PAIR + i * 256 + lane * 4) = lo16(p);
        *(LAS float*)(L + L_PAIR + (i + 1) * 256 + lane * 4) = hi16(p);
      }
      asm volatile("s_waitcnt lgkmcnt(0)" ::: "memory");
      *(volatile LAS u32*)(L + L_GO) = tag;
      bool bad = __builtin_amdgcn_ballot_w64(fa != tag) != 0ull;
      while (bad & (budget > 0)) {
        asm volatile("" ::: "memory");
        bad = __builtin_amdgcn_ballot_w64(ld32c(rws, WS_FA + rank * 1024 + (lane & 31) * 32, 0) != tag) != 0ull;
        --budget;
      }
      *(volatile LAS u32*)(L + L_GO + 4) = tag;
    }
    st32c(tag, rws, fa_own, 4);
    if (!packer) st32c(tag, rws, lane == 0 ? WS_FH + cl * 1024 : OOB, 0);
    STAMP(2);
    if (packer) {
      SB();
      if (rank < 7) {
#pragma unroll
        for (int u = 0; u < 4; ++u) {
          v4u f0, f1;
          xpose(rd + 8 * u, f0, f1);
          const int dst = WS_P2 + rank * 229376 + (pk_ * 4 + u) * 2048;
          st128c(f0, rws, l16, dst);
          st128c(f1, rws, l16 + 1024, dst);
        }
      } else {
#pragma unroll
        for (int i = 0; i < 4; ++i) *(LAS u32*)(L + L_SC + i * 256 + lane * 4) = pk(silu(lo16(cv[i])), silu(hi16(cv[i])));
      }
    }
    STAMP(3);
#pragma unroll
    for (int n = 0; n < NQW; ++n) wq_load(n);
    consts();
    drain_seen();
    STAMP(4);
  } else {
    const int v = w - 1;
    if (!packer) {
      const int uq = xcd + 8 * (j >> 4), ukh = (j >> 3) & 1;
      if (uq < 14) {
        u32 rm[16];
        const int vo = (ukh * 224 + v * 32 + g * 8) * 5376 + uq * 128 + c * 4;
        const u32 cv = ld16(rc, ukh * 448 + v * 64 + lane * 2, 0);
#pragma unroll
        for (int i = 0; i < 16; ++i) rm[i] = ld32(rwm, vo + (i >> 3) * 64, (i & 7) * 5376);
        SB();
        const float sv = silu(lo16(cv));
        *(LAS unsigned short*)(L + L_SC + v * 128 + lane * 2) = (unsigned short)pk(sv, sv);
        const v4u sf = lds128(L, L_SC + v * 128 + g * 16);
#pragma unroll
        for (int hh = 0; hh < 2; ++hh) {
          v4u f0, f1;
          xpose(rm + 8 * hh, f0, f1);
          const f32x4 a0 = mfma(f0, sf, z4), a1 = mfma(f1, sf, z4);
          if (c == 0) {
            stsf4(L, L_MODP + v * 256 + hh * 128 + g * 32, a0);
            stsf4(L, L_MODP + v * 256 + hh * 128 + g * 32 + 16, a1);
          }
        }
        asm volatile("s_waitcnt lgkmcnt(0)" ::: "memory");
        *(volatile LAS u32*)(L + L_ARR + v * 4) = tag;
        if (v == 0) {
          while ((__builtin_amdgcn_ballot_w64(*(volatile LAS u32*)(L + L_ARR + (lane < 7 ? lane : 0) * 4) != tag) != 0ull) & (budget > 0)) --budget;
          asm volatile("" ::: "memory");
          float s0 = 0.f;
#pragma unroll
          for (int k = 0; k < 7; ++k) s0 += *(LAS float*)(L + L_MODP + k * 256 + (lane >> 3) * 32 + (lane & 1) * 16 + (lane & 6) * 2);
          st64c(v2u{asu(s0), tag}, rws, WS_MU + (uq * 64 + lane) * 16 + ukh * 8, 0);
        }
      }
    }
    STAMP(1);
    int xsl[12], xrow[12];
    xsl[0] = v + 7 * ((rank * 3) >> 1);
#pragma unroll
    for (int i = 1; i < 12; ++i) xsl[i] = xsl[i - 1] + 7 >= 84 ? v : xsl[i - 1] + 7;
#pragma unroll
    for (int i = 0; i < 12; ++i) xrow[i] = xsl[i] < 78 ? xsl[i] + 2 : xsl[i] & 1;
    v4u xr[12];
#pragma unroll
    for (int i = 0; i < 12; ++i) {
      int tok = t0 - 16 + xrow[i];
      tok = tok < 0 ? 0 : tok;
      xr[i] = ld128(rx, lane < 56 ? l16 : OOB, tok * 896);
    }
    if (w == 7 && packer) {
      const int q = rank * 28 + pk_;
      const u64 wa = (u64)pwo, wm = (u64)pwm;
      dma4(v4u{(u32)wa, (u32)(wa >> 32) & 0xFFFFu, (u32)(D * D * 2), 0x00020000u}, lane < 14 ? q * 1792 + lane * 128 : OOB, L_DUMMY);
      dma4(v4u{(u32)wm, (u32)(wm >> 32) & 0xFFFFu, (u32)(D * 6 * D * 2), 0x00020000u},
           lane < 56 ? (2 * q + (lane >= 28)) * 5376 + 1792 + (lane >= 28 ? lane - 28 : lane) * 128 : OOB, L_DUMMY);
    }
    SB();
    consts();
    SB();
    f32x2 t[12][4];
    {
      float sm[12], sq[12];
#pragma unroll
      for (int i = 0; i < 12; ++i) {
        sm[i] = sq[i] = 0.f;
#pragma unroll
        for (int e = 0; e < 4; ++e) {
          sm[i] = dot2(xr[i][e], 0x3F803F80u, sm[i]);
          sq[i] = dot2(xr[i][e], xr[i][e], sq[i]);
        }
      }
      STAMP(2);
      float rs_[3], nn[3];
#pragma unroll
      for (int q = 0; q < 3; ++q) {
        const float m = rowsum(fold16(fold32(sm[4 * q], sm[4 * q + 2]), fold32(sm[4 * q + 1], sm[4 * q + 3]))) * (1.f / D);
        const float vr = rowsum(fold16(fold32(sq[4 * q], sq[4 * q + 2]), fold32(sq[4 * q + 1], sq[4 * q + 3]))) * (1.f / D);
        rs_[q] = __builtin_amdgcn_rsqf(fmaxf(vr - m * m, 0.f) + 1e-5f);
        nn[q] = -m * rs_[q];
        int r = xsl[4 * q] + 7 * g;
        r = r >= 84 ? r - 84 : r;
        if (c == 0) *(LAS float*)(L + L_MU1 + (r + 2) * 4) = m;
      }
#pragma unroll
      for (int i = 0; i < 12; ++i) {
        const float r = asf(__builtin_amdgcn_readlane(asu(rs_[i >> 2]), 16 * (i & 3)));
        float nv = asf(__builtin_amdgcn_readlane(asu(nn[i >> 2]), 16 * (i & 3)));
        asm("" : "+v"(nv));
#pragma unroll
        for (int e = 0; e < 4; ++e) {
          t[i][e] = fma2(up2(xr[i][e]), f32x2{r, r}, f32x2{nv, nv});
          asm volatile("" : "+v"(t[i][e]));
        }
      }
    }
    STAMP(3);
    SB();
    if (v < 4) {
      lds_wait(L, L_GO + 12, tag, budget);
#pragma unroll
      for (int i = 0; i < 14; i += 2) slab2(v * 14 + i);
      {
        const u64 xa = (u64)px;
        const v4u rsx = {(u32)xa, (u32)(xa >> 32) & 0xFFFFu, rank < 7 ? (u32)(S * D * 2) : 0u, 0x00020000u};
        const int vo = (lane >> 3) * 896 + rank * 128 + (((lane & 7) ^ (lane >> 3)) << 4);
        dma1kp(rsx, vo, (t0 + 16 * v) * 896, L_XR + v * 2048);
        dma1kp(rsx, vo, (t0 + 16 * v + 8) * 896, L_XR + v * 2048 + 1024);
      }
    }
    if (v == 6 && lane < 32) sts128(L, L_B + 57344 + l16, v4u{0u, 0u, 0u, 0u});
    if (v < 4) {
      const v4u sv = {SENT, SENT, SENT, SENT};
      st128c(sv, rws, l16, o_cl + (v * 16 + 2 * rank) * 1024);
      st128c(sv, rws, l16 + 1024, o_cl + (v * 16 + 2 * rank) * 1024);
      if (rank < 7) {
        st128c(sv, rws, l16, x_cl + (v * 14 + 2 * rank) * 1024);
        st128c(sv, rws, l16 + 1024, x_cl + (v * 14 + 2 * rank) * 1024);
        st128c(sv, rws, g == 0 ? l16 : OOB, x_cl + XS_OFF + (v * 7 + rank) * 256);
      }
    }
    lds_wait(L, L_GO, tag, budget);
    STAMP(4);
    const bool early = __builtin_amdgcn_readfirstlane(*(volatile LAS u32*)(L + L_GO + 4)) == tag;
    asm volatile("" ::: "memory");
    if (early) {
#pragma unroll
      for (int n = 0; n < NQW; ++n) wq_load(n);
    }
    SB();
    if (lane < 56) {
      const f32x4 ca[2] = {ldsf4(L, L_PAIR + 1792 + 2 * l16), ldsf4(L, L_PAIR + 1808 + 2 * l16)}, sa[2] = {ldsf4(L, L_PAIR + 2 * l16), ldsf4(L, L_PAIR + 16 + 2 * l16)};
      const int base = L_A + ((lane >> 2) * 64 + (lane & 3) * 16) * 16, l7 = (lane & 7) * 16;
#pragma unroll
      for (int i = 0; i < 12; ++i) {
        v4u hv;
        hv[0] = pk2(fma2(t[i][0], ca[0].xy, sa[0].xy));
        hv[1] = pk2(fma2(t[i][1], ca[0].zw, sa[0].zw));
        hv[2] = pk2(fma2(t[i][2], ca[1].xy, sa[1].xy));
        hv[3] = pk2(fma2(t[i][3], ca[1].zw, sa[1].zw));
        sts128(L, base + (l7 ^ ((xrow[i] & 15) * 16)) + (xrow[i] >> 4) * 14336, hv);
      }
    }
    SB();
    if (!early) {
      lds_wait(L, L_GO + 4, tag, budget);
#pragma unroll
      for (int n = 0; n < NQW; ++n) wq_load(n);
    }
    drain_seen();
  }
  bar();
  STAMP(5);
  if (rank == 7 && packer && w < 4) {
    const int wb = L_B + (8 * g + (c >> 2)) * 128 + ((w ^ ((c >> 3) | ((g & 1) << 1))) << 5) + (c & 3) * 8;
    f32x4 acc = z4;
#pragma unroll
    for (int s2 = 0; s2 < 2; ++s2) {
      v4u sf[7], af[7];
#pragma unroll
      for (int q = 0; q < 7; ++q) {
        const int s = 7 * s2 + q;
        const v2u lo = trread(L, wb + s * 4096), hi = trread(L, wb + s * 4096 + 512);
        af[q] = v4u{lo[0], lo[1], hi[0], hi[1]};
        sf[q] = lds128(L, L_SC + s * 64 + g * 16);
      }
#pragma unroll
      for (int q = 0; q < 7; ++q) acc = mfma(af[q], sf[q], acc);
    }
    st128c(v4u{pk(acc[0], acc[1]), tag, pk(acc[2], acc[3]), tag}, rws, c == 0 ? WS_MOD + pk_ * 256 + w * 64 + g * 16 : OOB, 0);
  }

  v4u w1f[28];
  const int w1o = WS_P1 + rank * 200704 + w * 28672, w1v = w < 7 ? l16 : OOB;
  auto w1_load = [&](int n) LAM { w1f[n] = ld128c(rws, w1v + (n & 3) * 1024, w1o + (n >> 2) * 4096); };
  f32x4 akv[5], aq[4];
  const int fbo = w != 0 ? OOB : WS_FB + cl * 128 + (lane & 31) * 4;
  u32 fb;
  {
#pragma unroll
    for (int r = 0; r < 5; ++r) akv[r] = f32x4{0.f, 0.f, 0.f, 0.f};
#pragma unroll
    for (int r = 0; r < 4; ++r) aq[r] = f32x4{0.f, 0.f, 0.f, 0.f};
    v4u hf[HFD];
    auto h_read = [&](int n) LAM { const int s = n / 5, r = n % 5; return lds128(L, ad[s & 1][(s >> 1) & 1] + r * 14336 + (s >> 1) * 1024); };
    u32 f1;
    const int f1o = lane < 14 ? WS_FA + rank * 1024 + (2 * lane + (w >> 2)) * 32 + 4 : OOB;
    const u64 f1m = w < 7 ? 0x3FFFull : 0ull;
#pragma unroll
    for (int n = 0; n < HFD; ++n) hf[n] = h_read(n);
    SB();
#pragma unroll
    for (int s = 0; s < 14; ++s) {
#pragma unroll
      for (int r = 0; r < 5; ++r) {
        akv[r] = mfma(wqf[3 * (s >> 1) + 2 * (s & 1)], hf[(5 * s + r) % HFD], akv[r]);
        if (!(s & 1) && r > 0) aq[r - 1] = mfma(wqf[3 * (s >> 1) + 1], hf[(5 * s + r) % HFD], aq[r - 1]);
        if (5 * s + r + HFD < 70) hf[(5 * s + r) % HFD] = h_read(5 * s + r + HFD);
        if (r == 0 && NQW + 3 * (s >> 1) + 2 * (s & 1) < 21) wq_load(NQW + 3 * (s >> 1) + 2 * (s & 1));
        if (r == 3 && !(s & 1) && NQW + 3 * (s >> 1) + 1 < 21) wq_load(NQW + 3 * (s >> 1) + 1);
        if (s == 0 && r == 4) st32c(tag, rws, lane != 0 || w > 4 ? OOB : w == 0 ? WS_FA + rank * 1024 + pk_ * 32 + 8 : WS_FB + cl * 128 + rank * 16 + (w - 1) * 4, 0);
        if (s == 11 && r == 0) fb = ld32c(rws, fbo, 0);
        if (s == 8 && r == 2) f1 = ld32c(rws, f1o, 0);
        if (s == 11 && r == 4) {
          if ((__builtin_amdgcn_ballot_w64(f1 != tag) & f1m) != 0ull) {
            bool bad;
            do {
              asm volatile("" ::: "memory");
              __builtin_amdgcn_s_sleep(PSLEEP);
              bad = (__builtin_amdgcn_ballot_w64(ld32c(rws, f1o, 0) != tag) & f1m) != 0ull;
              --budget;
            } while (bad & (budget > 0));
          }
          asm volatile("" ::: "memory");
        }
        if (s >= 12 && (r == 0 || r == 3)) w1_load(2 * (s - 12) + (r ? 1 : 0));
        SB();
      }
    }
  }
  if (w == 0) {
    bool fbad = __builtin_amdgcn_ballot_w64(fb != tag) != 0ull;
    while (fbad & (budget > 0)) {
      asm volatile("" ::: "memory");
      __builtin_amdgcn_s_sleep(PSLEEP);
      fbad = __builtin_amdgcn_ballot_w64(ld32c(rws, fbo, 0) != tag) != 0ull;
      --budget;
    }
  }
  const u64 wsa = (u64)pws;
  const v4u rsw = {(u32)wsa, (u32)(wsa >> 32) & 0xFFFFu, rank < 7 ? (u32)WS_END : 0u, 0x00020000u};
  STAMP(6);
  bar();
  const int mmo = w < 7 ? WS_MOD + w * 1024 + l16 : WS_FA + rank * 1024 + (lane & 31) * 32;
  v4u mm = ld128c(rws, mmo, 0);
#pragma unroll
  for (int n = 4; n < 8; ++n) w1_load(n);
  SB();
  {
    const int qb = L_A + ((w >> 1) & 1) * QK_HD + l16;
#pragma unroll
    for (int r = 0; r < 5; ++r) sts64(L, qb + (kh * 5 + r) * 1024 + (w & 1) * 8, v2u{pk(akv[r][0], akv[r][1]), pk(akv[r][2], akv[r][3])});
#pragma unroll
    for (int r = 0; r < 4; ++r) stsf4(L, qb + (10 + kh * 8 + 2 * r + (w & 1)) * 1024, aq[r]);
  }
  bar();
  {
    const int rt = w >> 1, hd = w & 1;
    const int qb = L_A + hd * QK_HD + l16 + rt * 1024;
    const int qq = L_A + hd * QK_HD + l16 + (10 + 2 * rt) * 1024;
    const f32x4 qa = ldsf4(L, qq) + ldsf4(L, qq + 8192), qc = ldsf4(L, qq + 1024) + ldsf4(L, qq + 9216);
    const v4u k0 = lds128(L, qb), k1 = lds128(L, qb + 1024);
    const int vb = L_A + hd * QK_HD + (5 + rt) * 1024 + ((c & 3) * 16 + 4 * g + (c >> 2)) * 16;
    const v2u v00 = trread(L, vb), v01 = trread(L, vb + 8), v10 = trread(L, vb + 1024), v11 = trread(L, vb + 1032);
    const v4u qf = {pk(qa[0], qa[1]), pk(qa[2], qa[3]), pk(qc[0], qc[1]), pk(qc[2], qc[3])};
    const f32x4 z = {0.f, 0.f, 0.f, 0.f};
    const f32x4 s0 = mfma(k0, qf, bias[0]), s1 = mfma(k1, qf, bias[1]);
#pragma unroll
    for (int n = 8; n < 12; ++n) w1_load(n);
    SB();
    const float sc = 0.18898223650461362f * 1.44269504088896341f;
    const float mx = -sc * gmax(fmaxf(fmaxf(fmaxf(s0[0], s0[1]), fmaxf(s0[2], s0[3])), fmaxf(fmaxf(s1[0], s1[1]), fmaxf(s1[2], s1[3]))));
    float sv[8];
    float sum = 0.f;
#pragma unroll
    for (int e = 0; e < 8; ++e) {
      sv[e] = __builtin_amdgcn_exp2f(vfma(e < 4 ? s0[e & 3] : s1[e & 3], sc, mx));
      sum += sv[e];
    }
    const float inv = __builtin_amdgcn_rcpf(gsum(sum));
    v4u pf;
#pragma unroll
    for (int i = 0; i < 4; ++i) pf[i] = pk(sv[2 * i] * inv, sv[2 * i + 1] * inv);
    const f32x4 o0 = mfma(v4u{v00[0], v00[1], v10[0], v10[1]}, pf, z), o1 = mfma(v4u{v01[0], v01[1], v11[0], v11[1]}, pf, z);
    v4u of;
#pragma unroll
    for (int i = 0; i < 4; ++i) of[i] = canon(pk(o0[i], o1[i]));
    st128c(of, rws, l16, o_cl + (rt * 16 + 2 * rank + hd) * 1024);
  }
  STAMP(7);
  if (w < 7) {
    while ((__builtin_amdgcn_ballot_w64((mm[1] != tag) | (mm[3] != tag)) != 0ull) & (budget > 0)) {
      asm volatile("" ::: "memory");
      __builtin_amdgcn_s_sleep(PSLEEP);
      mm = ld128c(rws, mmo, 0);
      --budget;
    }
    const int q = w * 64 + lane;
    const float one = (q >= 224 && q < 336) ? 1.f : 0.f;
    sts64(L, L_GA + (lane & ~7) * 8 + (lane & 3) * 16 + (lane & 4) * 2 + w * 512,
          v2u{pk(one + lo16(mm[0]), one + hi16(mm[0])), pk(one + lo16(mm[2]), one + hi16(mm[2]))});
  } else {
    while ((__builtin_amdgcn_ballot_w64(mm[2] != tag) != 0ull) & (budget > 0)) {
      asm volatile("" ::: "memory");
      __builtin_amdgcn_s_sleep(PSLEEP);
      mm = ld128c(rws, mmo, 0);
      --budget;
    }
  }
  SB();
  const int rt = w & 3, kx = kh ^ (rank & 1);
  const int pw = (w + rank) & 7;
  v4u x1p = {0u, 0u, 0u, 0u};
  if (rank < 7) {
    int wb[4];
#pragma unroll
    for (int x = 0; x < 4; ++x) wb[x] = L_B + (kx * 224 + 8 * g + (c >> 2)) * 128 + ((x ^ ((c >> 3) | ((g & 1) << 1))) << 5) + (c & 3) * 8;
    auto wo_read = [&](int q) LAM {
      const int hs = q >> 2, ct = q & 3;
      const v2u lo = trread(L, wb[ct ^ (((7 * hs) >> 1) & 1) * 2] + 28 * hs * 128);
      const v2u hi = trread(L, wb[ct ^ (((7 * hs + 1) >> 1) & 1) * 2] + (28 * hs + 4) * 128);
      return v4u{lo[0], lo[1], hi[0], hi[1]};
    };
    v4u wo[16];
#pragma unroll
    for (int q = 0; q < 16; ++q) wo[q] = wo_read(q);
    bar();
    const int vo = rank * 128 + kx * 64 + g * 16;
    const v4u ga = lds128(L, L_GA + vo), cb = lds128(L, L_CB + vo);
    const float mu1 = *(LAS float*)(L + L_MU1 + (16 + 16 * rt + c) * 4);
    v4u of[8];
    const int ovo = o_cl + (rt * 16 + 8 * kx) * 1024 + l16;
    issue<8>(of, rws, ovo);
    const int xo = L_XR + (16 * rt + c) * 128 + (g & 1) * 8;
    const v2u xr0 = *(LAS v2u*)(L + xo + (((kx * 4 + (g >> 1)) ^ (c & 7)) << 4)), xr1 = *(LAS v2u*)(L + xo + (((kx * 4 + 2 + (g >> 1)) ^ (c & 7)) << 4));
    SB();
    settle<8>(of, rws, ovo, budget);
    STAMP(8);
    f32x4 acc[4];
#pragma unroll
    for (int q = 0; q < 4; ++q) acc[q] = f32x4{0.f, 0.f, 0.f, 0.f};
#pragma unroll
    for (int q = 0; q < 32; ++q) {
      acc[q & 3] = mfma(wo[q & 15], of[q >> 2], acc[q & 3]);
      if (q < 16) wo[q] = wo_read(16 + q);
      if (q % 2 == 1 && 12 + q / 2 < N1C) w1_load(12 + q / 2);
      SB();
    }
#pragma unroll
    for (int q = 0; q < 4; ++q) stsf4(L, L_A + ((kx * 4 + rt) * 4 + q) * 1024 + l16, acc[q]);
    bar();
    const int pb = L_A + (rt * 4 + 2 * kx) * 1024 + l16;
    const f32x4 a0 = ldsf4(L, pb) + ldsf4(L, pb + 16384), a1 = ldsf4(L, pb + 1024) + ldsf4(L, pb + 17408);
    const v4u xres = {xr0[0], xr0[1], xr1[0], xr1[1]};
    v4u xp;
    f32x2 s1v = {0.f, 0.f}, s2v = {0.f, 0.f};
#pragma unroll
    for (int i = 0; i < 4; ++i) {
      x1p[i] = pk2(fma2(up2(ga[i]), i == 0 ? a0.xy : i == 1 ? a0.zw : i == 2 ? a1.xy : a1.zw, up2(xres[i])));
      const f32x2 d = up2(x1p[i]) - f32x2{mu1, mu1};
      s1v += d;
      s2v = fma2(d, d, s2v);
      xp[i] = canon(pk2(d * up2(cb[i])));
    }
    float s1 = s1v.x + s1v.y, s2 = s2v.x + s2v.y;
    st128c(xp, rws, l16, x_cl + (rt * 14 + 2 * rank + kx) * 1024);
    s1 = gsum(s1);
    s2 = gsum(s2);
    st64c(v2u{canon(asu(s1)), canon(asu(s2))}, rws, g == 0 ? l16 : OOB, x_cl + XS_OFF + (rt * 7 + rank) * 256 + kx * 8);
  } else {
    bar();
    const v4u sv = {SENT, SENT, SENT, SENT};
#pragma unroll 2
    for (int i = 0; i < 14; ++i) st128c(sv, rws, l16 + i * 1024, h_cl + w * 28672 + 7168);
    SB();
#pragma unroll
    for (int n = 12; n < N1C; ++n) w1_load(n);
    waitvm<N1C - 12>();
    bar();
    if (tid == 0) st32c(tag, rws, WS_FH + cl * 1024 + 4, 0);
    drain_seen();
  }
  STAMP(9);
  v4u w2f[28];
  f32x4 u0 = z4, u1 = z4;
  const int w2o = WS_P2 + rank * 229376 + pw * 28672, w2v = rank < 7 ? l16 : OOB;
  auto w2_load = [&](int n) LAM { w2f[n] = ld128c(rws, w2v + (n & 3) * 1024, w2o + (n >> 2) * 4096); };
  constexpr int L_W2 = 57344, NST = 28 - N2B;
  static_assert(L_W2 + 8 * NST * 1024 <= L_GA, "W2 staging");
  const v4u rsh = {(u32)wsa, (u32)(wsa >> 32) & 0xFFFFu, (u32)WS_END, 0x00020000u};
  {
    v4u xf[7], xs[1];
    const int pr = 4 * kx + g;
    const int xo = x_cl + (rt * 14 + 7 * kx) * 1024 + l16, so = pr < 7 ? x_cl + XS_OFF + (rt * 7 + pr) * 256 + c * 16 : OOB;
    const int vb = ((c & 1) ? L_SB : L_CB) + g * 16;
    {
      v4u bfv[N1C / 2];
#pragma unroll
      for (int s = 0; s < N1C / 2; ++s) bfv[s] = lds128(L, vb + s * 64);
      SB();
#pragma unroll
      for (int s = 0; s < N1C / 2; ++s) {
        u0 = mfma(w1f[2 * s], bfv[s], u0);
        u1 = mfma(w1f[2 * s + 1], bfv[s], u1);
      }
    }
    SB();
    bar();
    SB();
    issue<7>(xf, rws, xo);
    issue<1>(xs, rws, so);
    const int hro = w == 0 ? WS_FH + cl * 1024 + (lane & 1) * 4 : OOB;
    u32 hr = ld32c(rws, hro, 0);
    SB();
    settle<7>(xf, rws, xo, budget);
    settle<1>(xs, rws, so, budget);
    if (w == 0) {
      while ((__builtin_amdgcn_ballot_w64(hr != tag) != 0ull) & (budget > 0)) {
        asm volatile("" ::: "memory");
        __builtin_amdgcn_s_sleep(PSLEEP);
        hr = ld32c(rws, hro, 0);
        --budget;
      }
    }
#pragma unroll
    for (int n = N1C; n < 28; ++n) w1_load(n);
    SB();
#pragma unroll
    for (int i = 0; i < 7; ++i) sts128(L, L_A + (rt * 14 + 7 * kx + i) * 1024 + l16, xf[i]);
    const float s1 = gsum(asf(xs[0][0]) + asf(xs[0][2])), s2 = gsum(asf(xs[0][1]) + asf(xs[0][3]));
    if (g == 0) sts64(L, L_ST + ((rt * 2 + kx) * 16 + c) * 8, v2u{asu(s1), asu(s2)});
    SB();
#pragma unroll
    for (int n = 0; n < 4; ++n) w2_load(n);
  }
  STAMP(10);
  bar();
  STAMP(11);
  v4u hf[4][7];
  const int ho = rank < 7 ? h_cl + pw * 28672 + l16 : OOB;
  if (w < 7) {
    v4u xf[4];
#pragma unroll
    for (int i = 0; i < 4; ++i) xf[i] = lds128(L, L_A + i * 1024 + l16);
    const int ub = L_UV + w * 256 + g * 64;
    const int vb = ((c & 1) ? L_SB : L_CB) + g * 16;
    f32x4 uu[2], vv[2];
    SB();
    const float nl2e = -1.44269504f;
#pragma unroll
    for (int r = 0; r < 4; ++r) {
      f32x4 a[2] = {z4, z4};
      v2u sta, stb;
#pragma unroll
      for (int s = 0; s < 14; ++s) {
        const int n = r * 14 + s;
        a[0] = mfma(w1f[2 * s], xf[n & 3], a[0]);
        a[1] = mfma(w1f[2 * s + 1], xf[n & 3], a[1]);
        if (n + 4 < 56) xf[n & 3] = lds128(L, L_A + (n + 4) * 1024 + l16);
        if (s == 8) {
          sta = *(LAS v2u*)(L + L_ST + (r * 32 + c) * 8);
          stb = *(LAS v2u*)(L + L_ST + (r * 32 + 16 + c) * 8);
        }
        if (r == 0 && s >= N1C / 2) {
          const v4u bv = lds128(L, vb + s * 64);
          u0 = mfma(w1f[2 * s], bv, u0);
          u1 = mfma(w1f[2 * s + 1], bv, u1);
        }
        if (n >= 4 && n % 2 == 0 && 4 + (n - 4) / 2 < N2B) w2_load(4 + (n - 4) / 2);
        if (r == 3 && s >= 7) hf[0][s - 7] = ld128c(rws, ho + ((s - 7) & 3) * 1024, ((s - 7) >> 2) * 4096);
        SB();
      }
      if (r == 0) {
        const float sc = (c & 1) ? nl2e : 1.f;
        if (c < 2) {
          stsf4(L, ub + c * 32, u0 * sc);
          stsf4(L, ub + c * 32 + 16, u1 * sc);
        }
        uu[0] = ldsf4(L, ub);
        uu[1] = ldsf4(L, ub + 16);
        vv[0] = ldsf4(L, ub + 32);
        vv[1] = ldsf4(L, ub + 48);
      }
      const float mu = (asf(sta[0]) + asf(stb[0])) * (1.f / D);
      const float var = fmaxf((asf(sta[1]) + asf(stb[1])) * (1.f / D) - mu * mu, 0.f);
      const float rsl = nl2e * __builtin_amdgcn_rsqf(var + 1e-5f);
      float y[8];
#pragma unroll
      for (int e = 0; e < 8; ++e) {
        const float z = vfma(vfma(-mu, uu[e & 1][e >> 1], a[e & 1][e >> 1]), rsl, vv[e & 1][e >> 1]);
        y[e] = z * __builtin_amdgcn_rcpf(vfma(__builtin_amdgcn_exp2f(z), nl2e, nl2e));
      }
      const v4u hv = {canon(pkn(y[0], y[1])), canon(pkn(y[2], y[3])), canon(pkn(y[4], y[5])), canon(pkn(y[6], y[7]))};
      if (r == 3) {
        lds_wait(L, L_GO + 8, tag, budget);
      }
      st128c(hv, rws, l16, h_cl + (rank * 28 + w + r * 7) * 1024);
      SB();
    }
  } else {
    if (rank == 7) {
      const v4u sv = {SENT, SENT, SENT, SENT};
#pragma unroll 1
      for (int p = 0; p < 8; ++p) {
#pragma unroll
        for (int i = 0; i < 7; ++i) st128c(sv, rws, l16 + i * 1024, h_cl + p * 28672 + 21504);
      }
      drain();
      st32c(tag, rws, lane == 0 ? WS_FH + cl * 1024 + 8 : OOB, 0);
      *(volatile LAS u32*)(L + L_GO + 8) = tag;
    }
#pragma unroll
    for (int n = 4; n < N2B; ++n) w2_load(n);
    if (rank < 7) {
      bool seen = false;
#pragma unroll 1
      for (int ww = 0; ww < 8; ++ww) {
        u32 f;
        asm volatile("buffer_load_dword %0, off, %1, %2 sc1" : "=v"(f) : "s"(rsw), "s"(WS_FH + cl * 1024 + 8) : "memory");
#pragma unroll
        for (int q = 0; q < NST; ++q) dma1k(rsw, l16, WS_P2 + rank * 229376 + (ww * 28 + N2B + q) * 1024, L_W2 + (ww * NST + q) * 1024);
        asm volatile("s_waitcnt vmcnt(%1)" : "+v"(f) : "n"(NST) : "memory");
        if (__builtin_amdgcn_readfirstlane(f) == tag) {
          *(volatile LAS u32*)(L + L_GO + 8) = tag;
          seen = true;
        }
      }
      while (!seen & (budget > 0)) {
        asm volatile("" ::: "memory");
        seen = __builtin_amdgcn_readfirstlane(ld32c(rws, WS_FH + cl * 1024 + 8, 0)) == tag;
        --budget;
      }
      *(volatile LAS u32*)(L + L_GO + 8) = tag;
    }
    drain_seen();
  }
  if (w == 7) issue<7>(hf[0], rws, ho);
  STAMP(12);
  if (rank < 7) {
    bar();
    issue<7>(hf[1], rws, ho + 7168);
#pragma unroll
    for (int n = 0; n < NST; ++n) w2f[N2B + n] = lds128(L, L_W2 + (pw * NST + n) * 1024 + l16);
    SB();
#pragma unroll
    for (int r = 0; r < 4; ++r) {
      settle<7>(hf[r], rws, ho + r * 7168, budget);
      if (r < 2) issue<7>(hf[r + 2], rws, ho + (r + 2) * 7168);
      SB();
      f32x4 acc[4];
#pragma unroll
      for (int q = 0; q < 4; ++q) acc[q] = z4;
#pragma unroll
      for (int s = 0; s < 7; ++s) {
#pragma unroll
        for (int q = 0; q < 4; ++q) acc[q] = mfma(w2f[4 * s + q], hf[r][s], acc[q]);
      }
      SB();
      if (r == 1) bar();
#pragma unroll
      for (int q = 0; q < 4; ++q) stsf4(L, ((r * 8 + w) * 4 + q) * 1024 + l16, acc[q]);
      SB();
    }
    STAMP(13);
    const v4u gb = lds128(L, L_GB + rank * 128 + kx * 64 + g * 16);
    bar();
    f32x4 pa[8][2];
#pragma unroll
    for (int p = 0; p < 8; ++p) {
      pa[p][0] = ldsf4(L, ((rt * 8 + p) * 4 + 2 * kx) * 1024 + l16);
      pa[p][1] = ldsf4(L, ((rt * 8 + p) * 4 + 2 * kx + 1) * 1024 + l16);
    }
    SB();
    f32x4 a0 = pa[0][0], a1 = pa[0][1];
#pragma unroll
    for (int p = 1; p < 8; ++p) {
      a0 += pa[p][0];
      a1 += pa[p][1];
    }
    v4u ov;
#pragma unroll
    for (int i = 0; i < 4; ++i) {
      ov[i] = pkn(vfma(lo16(gb[i]), a0[i], lo16(x1p[i])), vfma(hi16(gb[i]), a1[i], hi16(x1p[i])));
    }
    const rsrc_t rout = mkrs(pout, S * D * 2);
    const auto e0 = __builtin_amdgcn_permlane16_swap(ov[0], ov[2], false, false), e1 = __builtin_amdgcn_permlane16_swap(ov[1], ov[3], false, false);
    const int oo = c * 896 + (g & 1) * 32 + (g >> 1) * 16 + (t0 + 16 * rt) * 896 + rank * 128 + kx * 64;
    __builtin_amdgcn_raw_buffer_store_b128(v4u{e0[0], e1[0], e0[1], e1[1]}, rout, oo, 0, 0);
  } else if (cl == 0 && w == 7) {
    for (;;) {
      asm volatile("" ::: "memory");
      bool ok = true;
#pragma unroll
      for (int i = 0; i < 4; ++i) {
        const v4u f = ld128c(rws, l16 + i * 1024, WS_FB);
        ok = ok & (f[0] == tag) & (f[1] == tag) & (f[2] == tag) & (f[3] == tag);
      }
      --budget;
      if ((__builtin_amdgcn_ballot_w64(!ok) == 0ull) | (budget <= 0)) break;
      __builtin_amdgcn_s_sleep(8);
    }
    if (lane == 0) st32c(tag, rws, WS_EPOCH, 0);
  }
  STAMP(14);
  asm volatile(".Lfk_pc1:");
}
}

extern "C" void kernel_launch(void* const* d_in, const int* in_sizes, int n_in, void* d_out, int out_size, void* d_ws,
                              size_t ws_size, hipStream_t stream) {
  (void)hipFuncSetAttribute((const void*)fk::fused, hipFuncAttributeMaxDynamicSharedMemorySize, fk::L_END);
  fk::fused<<<256, 512, fk::L_END, stream>>>(d_in[0], d_in[1], d_in[2], d_in[4], d_in[5], d_in[6], d_in[7], d_out, d_ws);
}
```

```cpp
#include <hip/hip_runtime.h>

typedef unsigned u32;
typedef unsigned long long u64;
typedef u32 v4u __attribute__((ext_vector_type(4)));
typedef u32 v2u __attribute__((ext_vector_type(2)));
typedef float f32x4 __attribute__((ext_vector_type(4)));
typedef float f32x2 __attribute__((ext_vector_type(2)));
typedef short bf16x8 __attribute__((ext_vector_type(8)));
typedef __bf16 bf16x2 __attribute__((ext_vector_type(2)));
typedef __amdgpu_buffer_rsrc_t rsrc_t;
#define LAS __attribute__((address_space(3)))
#define SB() __builtin_amdgcn_sched_barrier(0)
#define DEV __device__ __forceinline__
#define LAM __attribute__((always_inline))
#define fmaxf __builtin_fmaxf

namespace fk {
constexpr int S = 2048, D = 448, F = 1792;
constexpr int WS_EPOCH = 0;
constexpr int WS_FA = 81920;
constexpr int WS_FB = 8192;
constexpr int WS_FH = 32768;
constexpr int WS_MOD = 16384;
constexpr int WS_MU = 65536;
constexpr int WS_O = 131072;
constexpr int WS_X = WS_O + 32 * 65536;
constexpr int XS_OFF = 57344;
constexpr int WS_H = WS_X + 32 * 65536;
constexpr int H_CL = 8 * 4 * 7 * 1024;
constexpr int WS_PQ = WS_H + 32 * H_CL;
constexpr int WS_P2 = WS_PQ + 8 * 168 * 1024;
constexpr int WS_P1 = WS_P2 + 8 * 224 * 1024;
constexpr int WS_END = WS_P1 + 8 * 196 * 1024;
constexpr int L_A = 0;
constexpr int L_B = 71680;
constexpr int L_PAIR = 137216;
constexpr int L_MODP = 141312;
constexpr int L_SC = 143360;
constexpr int L_XR = 146432;
constexpr int L_MU1 = 154688;
constexpr int L_GA = 155648, L_SB = L_GA + 896, L_CB = L_SB + 896, L_GB = L_CB + 896;
constexpr int L_ST = L_GB + 896;
constexpr int L_UV = L_ST + 1024;
constexpr int L_CNT = L_UV + 1792;
constexpr int L_GO = L_CNT + 16, L_ARR = L_CNT + 32;
constexpr int L_DUMMY = L_CNT + 64;
constexpr int L_END = L_DUMMY + 1024;
static_assert(L_END <= 163840, "LDS");
constexpr int QK_HD = 26 * 1024;
constexpr u32 SENT = 0xFFFFFFFFu;
constexpr int SPIN_CAP = 1 << 16;
constexpr int OOB = 0x40000000;
constexpr int CODE_SLICES = 8;
constexpr int PSLEEP = 4;
constexpr int HFD = 8;
constexpr int NQW = 4;
constexpr int N1C = 22;
constexpr int N2B = 16;

DEV float asf(u32 x) { return __builtin_bit_cast(float, x); }
DEV u32 asu(float x) { return __builtin_bit_cast(u32, x); }
DEV float lo16(u32 x) { return asf(x << 16); }
DEV float hi16(u32 x) { return asf(x & 0xFFFF0000u); }
DEV u32 pk(float a, float b) {
  f32x2 v = {a, b};
  return __builtin_bit_cast(u32, __builtin_convertvector(v, bf16x2));
}
DEV u32 pk2(f32x2 v) { return __builtin_bit_cast(u32, __builtin_convertvector(v, bf16x2)); }
DEV u32 pkn(float a, float b) {
  asm("" : "+v"(a));
  asm("" : "+v"(b));
  return pk(a, b);
}
DEV f32x2 up2(u32 x) { return f32x2{lo16(x), hi16(x)}; }
DEV f32x2 fma2(f32x2 a, f32x2 b, f32x2 c) { return __builtin_elementwise_fma(a, b, c); }
DEV u32 canon(u32 v) { return v < 0xFFFFFFFEu ? v : 0xFFFFFFFEu; }
DEV f32x4 mfma(v4u a, v4u b, f32x4 c) {
  return __builtin_amdgcn_mfma_f32_16x16x32_bf16(__builtin_bit_cast(bf16x8, a), __builtin_bit_cast(bf16x8, b), c, 0, 0, 0);
}
DEV float dot2(u32 a, u32 b, float c) {
  return __builtin_amdgcn_fdot2_f32_bf16(__builtin_bit_cast(bf16x2, a), __builtin_bit_cast(bf16x2, b), c, false);
}
DEV rsrc_t mkrs(const void* p, int bytes) { return __builtin_amdgcn_make_buffer_rsrc((void*)p, 0, bytes, 0x00020000); }
DEV u32 ld32(rsrc_t r, int v, int s) { return __builtin_amdgcn_raw_buffer_load_b32(r, v, s, 0); }
DEV v4u ld128(rsrc_t r, int v, int s) { return __builtin_amdgcn_raw_buffer_load_b128(r, v, s, 0); }
DEV u32 ld16(rsrc_t r, int v, int s) { return __builtin_amdgcn_raw_buffer_load_b16(r, v, s, 0); }
DEV u32 ld32c(rsrc_t r, int v, int s) { return __builtin_amdgcn_raw_buffer_load_b32(r, v, s, 16); }
DEV v4u ld128c(rsrc_t r, int v, int s) { return __builtin_amdgcn_raw_buffer_load_b128(r, v, s, 16); }
DEV void st128c(v4u x, rsrc_t r, int v, int s) { __builtin_amdgcn_raw_buffer_store_b128(x, r, v + s, 0, 16); }
DEV void st64c(v2u x, rsrc_t r, int v, int s) { __builtin_amdgcn_raw_buffer_store_b64(x, r, v, s, 16); }
DEV void st32c(u32 x, rsrc_t r, int v, int s) { __builtin_amdgcn_raw_buffer_store_b32(x, r, v, s, 16); }
DEV v4u lds128(LAS char* L, int off) { return *(LAS v4u*)(L + off); }
DEV f32x4 ldsf4(LAS char* L, int off) { return *(LAS f32x4*)(L + off); }
DEV void sts128(LAS char* L, int off, v4u x) { *(LAS v4u*)(L + off) = x; }
DEV void stsf4(LAS char* L, int off, f32x4 x) { *(LAS f32x4*)(L + off) = x; }
DEV void sts64(LAS char* L, int off, v2u x) { *(LAS v2u*)(L + off) = x; }
typedef short v4s __attribute__((ext_vector_type(4)));
DEV v2u trread(LAS char* L, int off) { return __builtin_bit_cast(v2u, __builtin_amdgcn_ds_read_tr16_b64_v4i16((LAS v4s*)(L + off))); }
DEV void bar() {
  asm volatile("s_waitcnt lgkmcnt(0)" ::: "memory");
  __builtin_amdgcn_s_barrier();
  asm volatile("" ::: "memory");
}
DEV void drain() { asm volatile("s_waitcnt vmcnt(0)" ::: "memory"); }
DEV void drain_seen() {
  __builtin_amdgcn_s_waitcnt(0x0F70);
  drain();
}
template <int N>
DEV void waitvm() { asm volatile("s_waitcnt vmcnt(%0)" ::"n"(N) : "memory"); }
DEV void dma1k(v4u rs, int voff, int soff, int lds) {
  asm volatile("s_mov_b32 m0, %2\n\ts_nop 3\n\tbuffer_load_dwordx4 %0, %1, %3 offen sc1 lds" ::"v"(voff), "s"(rs), "s"(lds), "s"(soff) : "memory");
}
DEV void dma1kp(v4u rs, int voff, int soff, int lds) {
  asm volatile("s_mov_b32 m0, %2\n\ts_nop 3\n\tbuffer_load_dwordx4 %0, %1, %3 offen lds" ::"v"(voff), "s"(rs), "s"(lds), "s"(soff) : "memory");
}
DEV void dma4(v4u rs, int voff, int lds) {
  asm volatile("s_mov_b32 m0, %2\n\ts_nop 3\n\tbuffer_load_dword %0, %1, 0 offen lds" ::"v"(voff), "s"(rs), "s"(lds) : "memory");
}
DEV void xpose(const u32* r, v4u& f0, v4u& f1) {
#pragma unroll
  for (int i = 0; i < 4; ++i) {
    f0[i] = __builtin_amdgcn_perm(r[2 * i + 1], r[2 * i], 0x05040100u);
    f1[i] = __builtin_amdgcn_perm(r[2 * i + 1], r[2 * i], 0x07060302u);
  }
}
template <int CTRL>
DEV float dppadd(float x) {
  return x + asf(__builtin_amdgcn_update_dpp(0, asu(x), CTRL, 0xF, 0xF, true));
}
DEV float rowsum(float x) {
  x = dppadd<0x128>(x);
  x = dppadd<0x124>(x);
  x = dppadd<0x122>(x);
  return dppadd<0x121>(x);
}
DEV float fold32(float a, float b) {
  auto r = __builtin_amdgcn_permlane32_swap(asu(a), asu(b), false, false);
  return asf(r[0]) + asf(r[1]);
}
DEV float fold16(float a, float b) {
  auto r = __builtin_amdgcn_permlane16_swap(asu(a), asu(b), false, false);
  return asf(r[0]) + asf(r[1]);
}
DEV float gsum(float x) { x = fold16(x, x); return fold32(x, x); }
DEV float gmax(float x) {
  auto r = __builtin_amdgcn_permlane16_swap(asu(x), asu(x), false, false);
  x = fmaxf(asf(r[0]), asf(r[1]));
  auto q = __builtin_amdgcn_permlane32_swap(asu(x), asu(x), false, false);
  return fmaxf(asf(q[0]), asf(q[1]));
}
DEV u32 umax(u32 a, u32 b) { return a > b ? a : b; }
DEV u32 max4(v4u x, u32 m) {
  m = umax(umax(x[0], x[1]), m);
  return umax(umax(x[2], x[3]), m);
}
template <int N>
DEV void issue(v4u (&f)[N], rsrc_t rs, int voff) {
#pragma unroll
  for (int i = 0; i < N; ++i) f[i] = ld128c(rs, voff + (i & 3) * 1024, (i >> 2) * 4096);
}
template <int N>
DEV bool pending(const v4u (&f)[N]) {
  u32 m = 0;
#pragma unroll
  for (int i = 0; i < N; ++i) m = max4(f[i], m);
  return __builtin_amdgcn_ballot_w64(m == SENT) != 0ull;
}
DEV bool pend1(v4u f) { return __builtin_amdgcn_ballot_w64(max4(f, 0u) == SENT) != 0ull; }
template <int N>
DEV void settle(v4u (&f)[N], rsrc_t rs, int voff, int& budget) {
  if (pending<N>(f)) {
    bool again;
    do {
      __builtin_amdgcn_s_sleep(PSLEEP);
      asm volatile("" ::: "memory");
#pragma unroll
      for (int i = 0; i < N; ++i)
        if (pend1(f[i])) f[i] = ld128c(rs, voff + (i & 3) * 1024, (i >> 2) * 4096);
      --budget;
      again = pending<N>(f) & (budget > 0);
    } while (again);
  }
}
DEV float vfma(float a, float b, float c) { return __builtin_fmaf(a, b, c); }
DEV void lds_wait(LAS char* L, int off, u32 val, int& budget) {
  if (__builtin_amdgcn_readfirstlane(*(volatile LAS u32*)(L + off)) != val) {
    bool bad;
    do {
      __builtin_amdgcn_s_sleep(1);
      bad = __builtin_amdgcn_readfirstlane(*(volatile LAS u32*)(L + off)) != val;
      --budget;
    } while (bad & (budget > 0));
  }
  asm volatile("" ::: "memory");
}
DEV float silu(float x) { return x * __builtin_amdgcn_rcpf(1.f + __builtin_amdgcn_exp2f(-1.44269504f * x)); }

__global__ void __launch_bounds__(512, 2)
fused(const void* __restrict__ px, const void* __restrict__ pwqkv, const void* __restrict__ pwo, const void* __restrict__ pw1,
      const void* __restrict__ pw2, const void* __restrict__ pc, const void* __restrict__ pwm, void* pout, void* pws) {
  extern __shared__ __attribute__((aligned(16))) char smem[];
  LAS char* L = (LAS char*)smem;
  const int tid = threadIdx.x, lane = tid & 63, w = __builtin_amdgcn_readfirstlane(tid >> 6);
  const int c = lane & 15, g = lane >> 4, l16 = lane * 16;
  const int b = blockIdx.x, xcd = b & 7, j = b >> 3, cl = xcd * 4 + (j >> 3), rank = j & 7, t0 = cl * 64;
  int budget = SPIN_CAP;

  if (w == 7) {
    u64 pcv;
    u32 clen;
    asm volatile("s_getpc_b64 %0\n.Lfk_pc0:\n\ts_mov_b32 %1, .Lfk_pc1-.Lfk_pc0" : "=s"(pcv), "=s"(clen));
    dma4(v4u{(u32)pcv, (u32)(pcv >> 32) & 0xFFFFu, clen, 0x00020000u}, lane * 128 + (j & (CODE_SLICES - 1)) * 8192, L_DUMMY);
  }
  if (tid < 16) *(LAS u32*)(L + L_CNT + tid * 4) = 0u;
  asm volatile("" ::"s"(px), "s"(pwqkv), "s"(pwo), "s"(pw1), "s"(pw2), "s"(pc), "s"(pwm), "s"(pout), "s"(pws));
  bar();
  const rsrc_t rws = mkrs(pws, WS_END);
  u32 tag = *(const __attribute__((address_space(4))) u32*)((const char*)pws + WS_EPOCH);
  auto make_tag = [&]() LAM {
    asm volatile("" : "+s"(tag));
    tag += 1u;
  };
  const int rr = (rank + xcd) & 7, pk_ = 4 * rr + (j >> 3);
  const bool packer = rr < 7;
  const f32x4 z4 = {0.f, 0.f, 0.f, 0.f};
  const rsrc_t rc = mkrs(pc, D * 2), rwm = mkrs(pwm, D * 6 * D * 2), rx = mkrs(px, S * D * 2);
  const int o_cl = WS_O + cl * 65536, x_cl = WS_X + cl * 65536, h_cl = WS_H + cl * H_CL;
  const int kh = w >> 2;
  v4u wqf[21];
  const int pe = WS_PQ + ((rank * 12 + 4 + w) * 14 + 7 * kh) * 1024, po = WS_PQ + ((rank * 12 + 4 + w) * 14 + 7 - 7 * kh) * 1024;
  const int pq = WS_PQ + ((rank * 12 + (w & 3)) * 14 + 7 * kh) * 1024;
  auto wq_load = [&](int n) LAM {
    const int i = n / 3, k = n - 3 * i;
    wqf[n] = ld128c(rws, l16 + (i & 3) * 1024, (k == 0 ? pe : k == 1 ? pq : po) + (i >> 2) * 4096);
  };
  int ad[2][2];
  f32x4 bias[2];
  auto consts = [&]() LAM {
    const int hb0 = L_A + (g * 16 + (c ^ g)) * 16, hb1 = L_A + (g * 16 + (c ^ (4 + g))) * 16;
    ad[0][0] = (kh ? hb1 : hb0) + kh * 7168;
    ad[0][1] = (kh ? hb0 : hb1) + kh * 7168;
    ad[1][0] = (kh ? hb0 : hb1) + (1 - kh) * 7168;
    ad[1][1] = (kh ? hb1 : hb0) + (1 - kh) * 7168;
    const int tq = t0 + 16 * (w >> 1) + c;
#pragma unroll
    for (int e = 0; e < 8; ++e) {
      const int delta = c + (e < 4 ? 16 : 0) - 4 * g - (e & 3);
      bias[e >> 2][e & 3] = ((delta & ~14) == 0 && delta <= tq) ? 0.f : -1.0e30f;
    }
    asm volatile("" : "+v"(bias[0]), "+v"(bias[1]), "+v"(ad[0][0]), "+v"(ad[0][1]), "+v"(ad[1][0]), "+v"(ad[1][1]));
  };
  auto slab2 = [&](int i) LAM {
    const u64 wa = rank < 7 ? (u64)pwo : (u64)pwm;
    const int st = rank < 7 ? 896 : 5376;
    const v4u rso = {(u32)wa, (u32)(wa >> 32) & 0xFFFFu, rank < 7 ? (u32)(D * D * 2) : packer ? (u32)(D * 6 * D * 2) : 0u, 0x00020000u};
    const int r8 = lane >> 3, ch = lane & 7;
    const int v0 = r8 * st + (rank < 7 ? rank * 128 : 1792 + pk_ * 128) + ((ch ^ (r8 & 2)) << 4), v1 = v0 ^ 64;
    dma1kp(rso, v0, i * 8 * st, L_B + i * 1024);
    dma1kp(rso, v1, (i + 1) * 8 * st, L_B + (i + 1) * 1024);
  };
  if (w == 0) {
    const int ks = pk_ >> 1;
    const int fa_own = lane == 0 ? WS_FA + rank * 1024 + pk_ * 32 : OOB;
    u32 rb[32];
    if (packer) {
      u32 ra[24];
      const rsrc_t rq = mkrs(pwqkv, D * 3 * D * 2), r1 = mkrs(pw1, D * F * 2);
      const int vo = c < 14 ? g * 8 * 2688 + c * 4 : OOB;
#pragma unroll
      for (int u = 0; u < 3; ++u) {
        const int pr = 3 * (pk_ & 1) + u, so = ks * 32 * 2688 + (pr >> 1) * 896 + (pr & 1) * 56 + rank * 112;
#pragma unroll
        for (int jj = 0; jj < 8; ++jj) ra[8 * u + jj] = ld32(rq, vo + (jj & 1) * 2688, so + (jj >> 1) * 5376);
      }
      SB();
#pragma unroll
      for (int u = 0; u < 3; ++u) {
        v4u f0, f1;
        xpose(ra + 8 * u, f0, f1);
        const int dst = WS_PQ + ((rank * 12 + (3 * (pk_ & 1) + u) * 2) * 14 + ks) * 1024;
        st128c(f0, rws, l16, dst);
        st128c(f1, rws, l16, dst + 14336);
      }
      SB();
      const int v1 = g * 4 * 3584 + c * 4, v1l = (pk_ & 1) ? OOB : v1, s1 = ks * 32 * 3584 + rank * 448 + (pk_ & 1) * 256;
#pragma unroll
      for (int i = 0; i < 32; ++i) rb[i] = ld32(r1, (i < 24 ? v1 : v1l) + (i & 1) * 3584 + (i >> 3) * 64, s1 + ((i & 2) + ((i >> 2) & 1) * 16) * 3584);
      waitvm<32>();
    }
    make_tag();
    st32c(tag, rws, fa_own, 0);
    if (!packer) {
      __builtin_amdgcn_s_sleep(64);
      const v4u sv = {SENT, SENT, SENT, SENT};
#pragma unroll 1
      for (int p = 0; p < 8; ++p) {
#pragma unroll
        for (int i = 0; i < 7; ++i) st128c(sv, rws, l16 + i * 1024, h_cl + p * 28672);
      }
    }
    u32 rd[32];
    if (packer) {
#pragma unroll
      for (int u = 0; u < 4; ++u) {
        v4u f0, f1;
        xpose(rb + 8 * u, f0, f1);
        const int dst = WS_P1 + rank * 200704 + ((4 * (pk_ & 1) + u) * 14 + ks) * 2048, vo1 = (u == 3 && (pk_ & 1)) ? OOB : l16;
        st128c(f0, rws, vo1, dst);
        st128c(f1, rws, vo1 + 1024, dst);
      }
    }
    SB();
    u32 cv[4];
#pragma unroll
    for (int i = 0; i < 4; ++i) cv[i] = ld32(rc, rank == 7 ? lane * 4 + i * 256 : OOB, 0);
    {
      v4u m[14];
      u32 fa;
#pragma unroll
      for (int i = 0; i < 14; ++i) m[i] = ld128c(rws, l16 + (i & 3) * 1024, WS_MU + (i >> 2) * 4096);
      fa = ld32c(rws, WS_FA + rank * 1024 + (lane & 31) * 32, 0);
      SB();
      *(volatile LAS u32*)(L + L_GO + 12) = 1u;
      SB();
      const rsrc_t r2 = mkrs(pw2, packer && rank < 7 ? F * D * 2 : 0);
      const int vo = g * 8 * 896 + rank * 128 + (c >> 2) * 8 + (c & 1) * 4 + (c & 2) * 16, so = pk_ * 64 * 896;
#pragma unroll
      for (int i = 0; i < 32; ++i) rd[i] = ld32(r2, vo + (i & 3) * 896 + ((i >> 3) & 1) * 64, so + ((i >> 4) * 32 + (i & 4)) * 896);
      SB();
      bool ok = true;
#pragma unroll
      for (int i = 0; i < 14; ++i) ok = ok & (m[i][1] == tag) & (m[i][3] == tag);
      if (__builtin_amdgcn_ballot_w64(!ok) != 0ull) {
        bool bad;
        do {
          asm volatile("" ::: "memory");
#pragma unroll
          for (int i = 0; i < 14; ++i) m[i] = ld128c(rws, l16 + (i & 3) * 1024, WS_MU + (i >> 2) * 4096);
          ok = true;
#pragma unroll
          for (int i = 0; i < 14; ++i) ok = ok & (m[i][1] == tag) & (m[i][3] == tag);
          --budget;
          bad = __builtin_amdgcn_ballot_w64(!ok) != 0ull;
        } while (bad & (budget > 0));
      }
#pragma unroll
      for (int i = 0; i < 14; i += 2) {
        u32 p = pk(asf(m[i][0]) + asf(m[i][2]), asf(m[i + 1][0]) + asf(m[i + 1][2]));
        if (i >= 6) p = pk((i > 6 ? 1.f : 0.f) + lo16(p), 1.f + hi16(p));
        *(LAS float*)(L + L_PAIR + i * 256 + lane * 4) = lo16(p);
        *(LAS float*)(L + L_PAIR + (i + 1) * 256 + lane * 4) = hi16(p);
      }
      asm volatile("s_waitcnt lgkmcnt(0)" ::: "memory");
      *(volatile LAS u32*)(L + L_GO) = 1u;
      bool bad = __builtin_amdgcn_ballot_w64(fa != tag) != 0ull;
      while (bad & (budget > 0)) {
        asm volatile("" ::: "memory");
        bad = __builtin_amdgcn_ballot_w64(ld32c(rws, WS_FA + rank * 1024 + (lane & 31) * 32, 0) != tag) != 0ull;
        --budget;
      }
      *(volatile LAS u32*)(L + L_GO + 4) = 1u;
    }
    st32c(tag, rws, fa_own, 4);
    if (!packer) st32c(tag, rws, lane == 0 ? WS_FH + cl * 1024 : OOB, 0);
    if (packer) {
      SB();
      if (rank < 7) {
#pragma unroll
        for (int u = 0; u < 4; ++u) {
          v4u f0, f1;
          xpose(rd + 8 * u, f0, f1);
          const int dst = WS_P2 + rank * 229376 + (pk_ * 4 + u) * 2048;
          st128c(f0, rws, l16, dst);
          st128c(f1, rws, l16 + 1024, dst);
        }
      } else {
#pragma unroll
        for (int i = 0; i < 4; ++i) *(LAS u32*)(L + L_SC + i * 256 + lane * 4) = pk(silu(lo16(cv[i])), silu(hi16(cv[i])));
      }
    }
#pragma unroll
    for (int n = 0; n < NQW; ++n) wq_load(n);
    consts();
    drain_seen();
  } else {
    const int v = w - 1;
    bool tagged = false;
    if (!packer) {
      const int uq = xcd + 8 * (j >> 4), ukh = (j >> 3) & 1;
      if (uq < 14) {
        u32 rm[16];
        const int vo = (ukh * 224 + v * 32 + g * 8) * 5376 + uq * 128 + c * 4;
        const u32 cv = ld16(rc, ukh * 448 + v * 64 + lane * 2, 0);
#pragma unroll
        for (int i = 0; i < 16; ++i) rm[i] = ld32(rwm, vo + (i >> 3) * 64, (i & 7) * 5376);
        SB();
        make_tag();
        tagged = true;
        const float sv = silu(lo16(cv));
        *(LAS unsigned short*)(L + L_SC + v * 128 + lane * 2) = (unsigned short)pk(sv, sv);
        asm volatile("" ::: "memory");
        const v4u sf = lds128(L, L_SC + v * 128 + g * 16);
#pragma unroll
        for (int hh = 0; hh < 2; ++hh) {
          v4u f0, f1;
          xpose(rm + 8 * hh, f0, f1);
          const f32x4 a0 = mfma(f0, sf, z4), a1 = mfma(f1, sf, z4);
          if (c == 0) {
            stsf4(L, L_MODP + v * 256 + hh * 128 + g * 32, a0);
            stsf4(L, L_MODP + v * 256 + hh * 128 + g * 32 + 16, a1);
          }
        }
        asm volatile("s_waitcnt lgkmcnt(0)" ::: "memory");
        *(volatile LAS u32*)(L + L_ARR + v * 4) = 1u;
        if (v == 0) {
          while ((__builtin_amdgcn_ballot_w64(*(volatile LAS u32*)(L + L_ARR + (lane < 7 ? lane : 0) * 4) != 1u) != 0ull) & (budget > 0)) --budget;
          asm volatile("" ::: "memory");
          float s0 = 0.f;
#pragma unroll
          for (int k = 0; k < 7; ++k) s0 += *(LAS float*)(L + L_MODP + k * 256 + (lane >> 3) * 32 + (lane & 1) * 16 + (lane & 6) * 2);
          st64c(v2u{asu(s0), tag}, rws, WS_MU + (uq * 64 + lane) * 16 + ukh * 8, 0);
        }
      }
    }
    int xsl[12], xrow[12];
    xsl[0] = v + 7 * ((rank * 3) >> 1);
#pragma unroll
    for (int i = 1; i < 12; ++i) xsl[i] = xsl[i - 1] + 7 >= 84 ? v : xsl[i - 1] + 7;
#pragma unroll
    for (int i = 0; i < 12; ++i) xrow[i] = xsl[i] < 78 ? xsl[i] + 2 : xsl[i] & 1;
    v4u xr[12];
#pragma unroll
    for (int i = 0; i < 12; ++i) {
      int tok = t0 - 16 + xrow[i];
      tok = tok < 0 ? 0 : tok;
      xr[i] = ld128(rx, lane < 56 ? l16 : OOB, tok * 896);
    }
    if (w == 7 && packer) {
      const int q = rank * 28 + pk_;
      const u64 wa = (u64)pwo, wm = (u64)pwm;
      dma4(v4u{(u32)wa, (u32)(wa >> 32) & 0xFFFFu, (u32)(D * D * 2), 0x00020000u}, lane < 14 ? q * 1792 + lane * 128 : OOB, L_DUMMY);
      dma4(v4u{(u32)wm, (u32)(wm >> 32) & 0xFFFFu, (u32)(D * 6 * D * 2), 0x00020000u},
           lane < 56 ? (2 * q + (lane >= 28)) * 5376 + 1792 + (lane >= 28 ? lane - 28 : lane) * 128 : OOB, L_DUMMY);
    }
    SB();
    if (!tagged) make_tag();
    consts();
    SB();
    f32x2 t[12][4];
    {
      float sm[12], sq[12];
#pragma unroll
      for (int i = 0; i < 12; ++i) {
        sm[i] = sq[i] = 0.f;
#pragma unroll
        for (int e = 0; e < 4; ++e) {
          sm[i] = dot2(xr[i][e], 0x3F803F80u, sm[i]);
          sq[i] = dot2(xr[i][e], xr[i][e], sq[i]);
        }
      }
      float rs_[3], nn[3];
#pragma unroll
      for (int q = 0; q < 3; ++q) {
        const float m = rowsum(fold16(fold32(sm[4 * q], sm[4 * q + 2]), fold32(sm[4 * q + 1], sm[4 * q + 3]))) * (1.f / D);
        const float vr = rowsum(fold16(fold32(sq[4 * q], sq[4 * q + 2]), fold32(sq[4 * q + 1], sq[4 * q + 3]))) * (1.f / D);
        rs_[q] = __builtin_amdgcn_rsqf(fmaxf(vr - m * m, 0.f) + 1e-5f);
        nn[q] = -m * rs_[q];
        int r = xsl[4 * q] + 7 * g;
        r = r >= 84 ? r - 84 : r;
        if (c == 0) *(LAS float*)(L + L_MU1 + (r + 2) * 4) = m;
      }
#pragma unroll
      for (int i = 0; i < 12; ++i) {
        const float r = asf(__builtin_amdgcn_readlane(asu(rs_[i >> 2]), 16 * (i & 3)));
        float nv = asf(__builtin_amdgcn_readlane(asu(nn[i >> 2]), 16 * (i & 3)));
        asm("" : "+v"(nv));
#pragma unroll
        for (int e = 0; e < 4; ++e) {
          t[i][e] = fma2(up2(xr[i][e]), f32x2{r, r}, f32x2{nv, nv});
          asm volatile("" : "+v"(t[i][e]));
        }
      }
    }
    SB();
    if (v < 4) {
      lds_wait(L, L_GO + 12, 1u, budget);
#pragma unroll
      for (int i = 0; i < 14; i += 2) slab2(v * 14 + i);
      {
        const u64 xa = (u64)px;
        const v4u rsx = {(u32)xa, (u32)(xa >> 32) & 0xFFFFu, rank < 7 ? (u32)(S * D * 2) : 0u, 0x00020000u};
        const int vo = (lane >> 3) * 896 + rank * 128 + (((lane & 7) ^ (lane >> 3)) << 4);
        dma1kp(rsx, vo, (t0 + 16 * v) * 896, L_XR + v * 2048);
        dma1kp(rsx, vo, (t0 + 16 * v + 8) * 896, L_XR + v * 2048 + 1024);
      }
    }
    if (v == 6 && lane < 32) sts128(L, L_B + 57344 + l16, v4u{0u, 0u, 0u, 0u});
    if (v < 4) {
      const v4u sv = {SENT, SENT, SENT, SENT};
      st128c(sv, rws, l16, o_cl + (v * 16 + 2 * rank) * 1024);
      st128c(sv, rws, l16 + 1024, o_cl + (v * 16 + 2 * rank) * 1024);
      if (rank < 7) {
        st128c(sv, rws, l16, x_cl + (v * 14 + 2 * rank) * 1024);
        st128c(sv, rws, l16 + 1024, x_cl + (v * 14 + 2 * rank) * 1024);
        st128c(sv, rws, g == 0 ? l16 : OOB, x_cl + XS_OFF + (v * 7 + rank) * 256);
      }
    }
    lds_wait(L, L_GO, 1u, budget);
    const bool early = __builtin_amdgcn_readfirstlane(*(volatile LAS u32*)(L + L_GO + 4)) == 1u;
    asm volatile("" ::: "memory");
    if (early) {
#pragma unroll
      for (int n = 0; n < NQW; ++n) wq_load(n);
    }
    SB();
    if (lane < 56) {
      const f32x4 ca[2] = {ldsf4(L, L_PAIR + 1792 + 2 * l16), ldsf4(L, L_PAIR + 1808 + 2 * l16)}, sa[2] = {ldsf4(L, L_PAIR + 2 * l16), ldsf4(L, L_PAIR + 16 + 2 * l16)};
      const int base = L_A + ((lane >> 2) * 64 + (lane & 3) * 16) * 16, l7 = (lane & 7) * 16;
#pragma unroll
      for (int i = 0; i < 12; ++i) {
        v4u hv;
        hv[0] = pk2(fma2(t[i][0], ca[0].xy, sa[0].xy));
        hv[1] = pk2(fma2(t[i][1], ca[0].zw, sa[0].zw));
        hv[2] = pk2(fma2(t[i][2], ca[1].xy, sa[1].xy));
        hv[3] = pk2(fma2(t[i][3], ca[1].zw, sa[1].zw));
        sts128(L, base + (l7 ^ ((xrow[i] & 15) * 16)) + (xrow[i] >> 4) * 14336, hv);
      }
    }
    SB();
    if (!early) {
      lds_wait(L, L_GO + 4, 1u, budget);
#pragma unroll
      for (int n = 0; n < NQW; ++n) wq_load(n);
    }
    drain_seen();
  }
  bar();
  if (rank == 7 && packer && w < 4) {
    const int wb = L_B + (8 * g + (c >> 2)) * 128 + ((w ^ ((c >> 3) | ((g & 1) << 1))) << 5) + (c & 3) * 8;
    f32x4 acc = z4;
#pragma unroll
    for (int s2 = 0; s2 < 2; ++s2) {
      v4u sf[7], af[7];
#pragma unroll
      for (int q = 0; q < 7; ++q) {
        const int s = 7 * s2 + q;
        const v2u lo = trread(L, wb + s * 4096), hi = trread(L, wb + s * 4096 + 512);
        af[q] = v4u{lo[0], lo[1], hi[0], hi[1]};
        sf[q] = lds128(L, L_SC + s * 64 + g * 16);
      }
#pragma unroll
      for (int q = 0; q < 7; ++q) acc = mfma(af[q], sf[q], acc);
    }
    st128c(v4u{pk(acc[0], acc[1]), tag, pk(acc[2], acc[3]), tag}, rws, c == 0 ? WS_MOD + pk_ * 256 + w * 64 + g * 16 : OOB, 0);
  }

  v4u w1f[28];
  const int w1o = WS_P1 + rank * 200704 + w * 28672, w1v = w < 7 ? l16 : OOB;
  auto w1_load = [&](int n) LAM { w1f[n] = ld128c(rws, w1v + (n & 3) * 1024, w1o + (n >> 2) * 4096); };
  f32x4 akv[5], aq[4];
  const int fbo = w != 0 ? OOB : WS_FB + cl * 128 + (lane & 31) * 4;
  u32 fb;
  {
#pragma unroll
    for (int r = 0; r < 5; ++r) akv[r] = f32x4{0.f, 0.f, 0.f, 0.f};
#pragma unroll
    for (int r = 0; r < 4; ++r) aq[r] = f32x4{0.f, 0.f, 0.f, 0.f};
    v4u hf[HFD];
    auto h_read = [&](int n) LAM { const int s = n / 5, r = n % 5; return lds128(L, ad[s & 1][(s >> 1) & 1] + r * 14336 + (s >> 1) * 1024); };
    u32 f1;
    const int f1o = lane < 14 ? WS_FA + rank * 1024 + (2 * lane + (w >> 2)) * 32 + 4 : OOB;
    const u64 f1m = w < 7 ? 0x3FFFull : 0ull;
#pragma unroll
    for (int n = 0; n < HFD; ++n) hf[n] = h_read(n);
    SB();
#pragma unroll
    for (int s = 0; s < 14; ++s) {
#pragma unroll
      for (int r = 0; r < 5; ++r) {
        akv[r] = mfma(wqf[3 * (s >> 1) + 2 * (s & 1)], hf[(5 * s + r) % HFD], akv[r]);
        if (!(s & 1) && r > 0) aq[r - 1] = mfma(wqf[3 * (s >> 1) + 1], hf[(5 * s + r) % HFD], aq[r - 1]);
        if (5 * s + r + HFD < 70) hf[(5 * s + r) % HFD] = h_read(5 * s + r + HFD);
        if (r == 0 && NQW + 3 * (s >> 1) + 2 * (s & 1) < 21) wq_load(NQW + 3 * (s >> 1) + 2 * (s & 1));
        if (r == 3 && !(s & 1) && NQW + 3 * (s >> 1) + 1 < 21) wq_load(NQW + 3 * (s >> 1) + 1);
        if (s == 0 && r == 4) st32c(tag, rws, lane != 0 || w > 4 ? OOB : w == 0 ? WS_FA + rank * 1024 + pk_ * 32 + 8 : WS_FB + cl * 128 + rank * 16 + (w - 1) * 4, 0);
        if (s == 11 && r == 0) fb = ld32c(rws, fbo, 0);
        if (s == 8 && r == 2) f1 = ld32c(rws, f1o, 0);
        if (s == 11 && r == 4) {
          if ((__builtin_amdgcn_ballot_w64(f1 != tag) & f1m) != 0ull) {
            bool bad;
            do {
              asm volatile("" ::: "memory");
              __builtin_amdgcn_s_sleep(PSLEEP);
              bad = (__builtin_amdgcn_ballot_w64(ld32c(rws, f1o, 0) != tag) & f1m) != 0ull;
              --budget;
            } while (bad & (budget > 0));
          }
          asm volatile("" ::: "memory");
        }
        if (s >= 12 && (r == 0 || r == 3)) w1_load(2 * (s - 12) + (r ? 1 : 0));
        SB();
      }
    }
  }
  if (w == 0) {
    bool fbad = __builtin_amdgcn_ballot_w64(fb != tag) != 0ull;
    while (fbad & (budget > 0)) {
      asm volatile("" ::: "memory");
      __builtin_amdgcn_s_sleep(PSLEEP);
      fbad = __builtin_amdgcn_ballot_w64(ld32c(rws, fbo, 0) != tag) != 0ull;
      --budget;
    }
  }
  const u64 wsa = (u64)pws;
  const v4u rsw = {(u32)wsa, (u32)(wsa >> 32) & 0xFFFFu, rank < 7 ? (u32)WS_END : 0u, 0x00020000u};
  v2u kvp[5];
#pragma unroll
  for (int r = 0; r < 5; ++r) {
    kvp[r] = v2u{pk(akv[r][0], akv[r][1]), pk(akv[r][2], akv[r][3])};
    asm volatile("" : "+v"(kvp[r]));
  }
  int xw = L_A + ((w >> 1) & 1) * QK_HD + l16 + kh * 5120 + (w & 1) * 8, xq = L_A + ((w >> 1) & 1) * QK_HD + l16 + (10 + kh * 8 + (w & 1)) * 1024;
  int qb = L_A + (w & 1) * QK_HD + l16 + (w >> 1) * 1024, qq = qb + (10 + (w >> 1)) * 1024;
  int vb_ = L_A + (w & 1) * QK_HD + (5 + (w >> 1)) * 1024 + ((c & 3) * 16 + 4 * g + (c >> 2)) * 16;
  asm volatile("" : "+v"(xw), "+v"(xq), "+v"(qb), "+v"(qq), "+v"(vb_));
  bar();
#pragma unroll
  for (int r = 0; r < 5; ++r) sts64(L, xw + r * 1024, kvp[r]);
#pragma unroll
  for (int r = 0; r < 4; ++r) stsf4(L, xq + r * 2048, aq[r]);
  SB();
  const int mmo = w < 7 ? WS_MOD + w * 1024 + l16 : WS_FA + rank * 1024 + (lane & 31) * 32;
  v4u mm = ld128c(rws, mmo, 0);
#pragma unroll
  for (int n = 4; n < 8; ++n) w1_load(n);
  SB();
  bar();
  {
    const int rt = w >> 1, hd = w & 1;
    const f32x4 qa = ldsf4(L, qq) + ldsf4(L, qq + 8192), qc = ldsf4(L, qq + 1024) + ldsf4(L, qq + 9216);
    const v4u k0 = lds128(L, qb), k1 = lds128(L, qb + 1024);
    const v2u v00 = trread(L, vb_), v01 = trread(L, vb_ + 8), v10 = trread(L, vb_ + 1024), v11 = trread(L, vb_ + 1032);
    const v4u qf = {pk(qa[0], qa[1]), pk(qa[2], qa[3]), pk(qc[0], qc[1]), pk(qc[2], qc[3])};
    const f32x4 z = {0.f, 0.f, 0.f, 0.f};
    const f32x4 s0 = mfma(k0, qf, bias[0]), s1 = mfma(k1, qf, bias[1]);
#pragma unroll
    for (int n = 8; n < 12; ++n) w1_load(n);
    SB();
    const float sc = 0.18898223650461362f * 1.44269504088896341f;
    const float mx = -sc * gmax(fmaxf(fmaxf(fmaxf(s0[0], s0[1]), fmaxf(s0[2], s0[3])), fmaxf(fmaxf(s1[0], s1[1]), fmaxf(s1[2], s1[3]))));
    float sv[8];
    float sum = 0.f;
#pragma unroll
    for (int e = 0; e < 8; ++e) {
      sv[e] = __builtin_amdgcn_exp2f(vfma(e < 4 ? s0[e & 3] : s1[e & 3], sc, mx));
      sum += sv[e];
    }
    v4u pf;
#pragma unroll
    for (int i = 0; i < 4; ++i) pf[i] = pk(sv[2 * i], sv[2 * i + 1]);
    const f32x4 o0 = mfma(v4u{v00[0], v00[1], v10[0], v10[1]}, pf, z), o1 = mfma(v4u{v01[0], v01[1], v11[0], v11[1]}, pf, z);
    const float inv = __builtin_amdgcn_rcpf(gsum(sum));
    v4u of;
#pragma unroll
    for (int i = 0; i < 4; ++i) of[i] = canon(pkn(o0[i] * inv, o1[i] * inv));
    st128c(of, rws, l16, o_cl + (rt * 16 + 2 * rank + hd) * 1024);
  }
  if (w < 7) {
    while ((__builtin_amdgcn_ballot_w64((mm[1] != tag) | (mm[3] != tag)) != 0ull) & (budget > 0)) {
      asm volatile("" ::: "memory");
      __builtin_amdgcn_s_sleep(PSLEEP);
      mm = ld128c(rws, mmo, 0);
      --budget;
    }
    const int q = w * 64 + lane;
    const float one = (q >= 224 && q < 336) ? 1.f : 0.f;
    sts64(L, L_GA + (lane & ~7) * 8 + (lane & 3) * 16 + (lane & 4) * 2 + w * 512,
          v2u{pk(one + lo16(mm[0]), one + hi16(mm[0])), pk(one + lo16(mm[2]), one + hi16(mm[2]))});
  } else {
    while ((__builtin_amdgcn_ballot_w64(mm[2] != tag) != 0ull) & (budget > 0)) {
      asm volatile("" ::: "memory");
      __builtin_amdgcn_s_sleep(PSLEEP);
      mm = ld128c(rws, mmo, 0);
      --budget;
    }
  }
  SB();
  const int rt = w & 3, kx = kh ^ (rank & 1);
  const int pw = (w + rank) & 7;
  v4u x1p = {0u, 0u, 0u, 0u};
  if (rank < 7) {
    int wb[4];
#pragma unroll
    for (int x = 0; x < 4; ++x) wb[x] = L_B + (kx * 224 + 8 * g + (c >> 2)) * 128 + ((x ^ (2 * kx) ^ ((c >> 3) | ((g & 1) << 1))) << 5) + (c & 3) * 8;
    auto wo_read = [&](int q) LAM {
      const int hs = q >> 2, ct = q & 3;
      const v2u lo = trread(L, wb[ct ^ (((7 * hs) >> 1) & 1) * 2] + 28 * hs * 128);
      const v2u hi = trread(L, wb[ct ^ (((7 * hs + 1) >> 1) & 1) * 2] + (28 * hs + 4) * 128);
      return v4u{lo[0], lo[1], hi[0], hi[1]};
    };
    v4u wo[16];
#pragma unroll
    for (int q = 0; q < 16; ++q) wo[q] = wo_read(q);
    bar();
    const int vo = rank * 128 + kx * 64 + g * 16;
    const v4u ga = lds128(L, L_GA + vo), cb = lds128(L, L_CB + vo);
    const float mu1 = *(LAS float*)(L + L_MU1 + (16 + 16 * rt + c) * 4);
    v4u of[8];
    const int ovo = o_cl + (rt * 16 + 8 * kx) * 1024 + l16;
    issue<8>(of, rws, ovo);
    const int xo = L_XR + (16 * rt + c) * 128 + (g & 1) * 8;
    const v2u xr0 = *(LAS v2u*)(L + xo + (((kx * 4 + (g >> 1)) ^ (c & 7)) << 4)), xr1 = *(LAS v2u*)(L + xo + (((kx * 4 + 2 + (g >> 1)) ^ (c & 7)) << 4));
    f32x2 gaf[4], cbf[4], xf2[4];
#pragma unroll
    for (int i = 0; i < 4; ++i) {
      gaf[i] = up2(ga[i]);
      cbf[i] = up2(cb[i]);
      xf2[i] = up2(i < 2 ? xr0[i & 1] : xr1[i & 1]);
      asm volatile("" : "+v"(gaf[i]), "+v"(cbf[i]), "+v"(xf2[i]));
    }
    SB();
    settle<8>(of, rws, ovo, budget);
    f32x4 acc[4];
#pragma unroll
    for (int q = 0; q < 4; ++q) acc[q] = f32x4{0.f, 0.f, 0.f, 0.f};
#pragma unroll
    for (int q = 0; q < 32; ++q) {
      acc[q & 3] = mfma(wo[q & 15], of[q >> 2], acc[q & 3]);
      if (q < 16) wo[q] = wo_read(16 + q);
      if (q % 2 == 1 && 12 + q / 2 < N1C) w1_load(12 + q / 2);
      SB();
    }
    stsf4(L, L_A + w * 2048 + l16, acc[2]);
    stsf4(L, L_A + w * 2048 + 1024 + l16, acc[3]);
    bar();
    const f32x4 a0 = acc[0] + ldsf4(L, L_A + (w ^ 4) * 2048 + l16), a1 = acc[1] + ldsf4(L, L_A + (w ^ 4) * 2048 + 1024 + l16);
    v4u xp;
    f32x2 s1v = {0.f, 0.f}, s2v = {0.f, 0.f};
#pragma unroll
    for (int i = 0; i < 4; ++i) {
      x1p[i] = pk2(fma2(gaf[i], i == 0 ? a0.xy : i == 1 ? a0.zw : i == 2 ? a1.xy : a1.zw, xf2[i]));
      const f32x2 d = up2(x1p[i]) - f32x2{mu1, mu1};
      s1v += d;
      s2v = fma2(d, d, s2v);
      xp[i] = canon(pk2(d * cbf[i]));
    }
    float s1 = s1v.x + s1v.y, s2 = s2v.x + s2v.y;
    st128c(xp, rws, l16, x_cl + (rt * 14 + 2 * rank + kx) * 1024);
    s1 = gsum(s1);
    s2 = gsum(s2);
    st64c(v2u{canon(asu(s1)), canon(asu(s2))}, rws, g == 0 ? l16 : OOB, x_cl + XS_OFF + (rt * 7 + rank) * 256 + kx * 8);
  } else {
    bar();
    const v4u sv = {SENT, SENT, SENT, SENT};
#pragma unroll 2
    for (int i = 0; i < 14; ++i) st128c(sv, rws, l16 + i * 1024, h_cl + w * 28672 + 7168);
    SB();
#pragma unroll
    for (int n = 12; n < N1C; ++n) w1_load(n);
    waitvm<N1C - 12>();
    bar();
    if (tid == 0) st32c(tag, rws, WS_FH + cl * 1024 + 4, 0);
    drain_seen();
  }
  v4u w2f[28];
  f32x4 u0 = z4, u1 = z4;
  const int w2o = WS_P2 + rank * 229376 + pw * 28672, w2v = rank < 7 ? l16 : OOB;
  auto w2_load = [&](int n) LAM { w2f[n] = ld128c(rws, w2v + (n & 3) * 1024, w2o + (n >> 2) * 4096); };
  constexpr int L_W2 = 57344, NST = 28 - N2B;
  static_assert(L_W2 + 8 * NST * 1024 <= L_GA, "W2 staging");
  {
    v4u xf[7], xs[1];
    const int pr = 4 * kx + g;
    const int xo = x_cl + (rt * 14 + 7 * kx) * 1024 + l16, so = pr < 7 ? x_cl + XS_OFF + (rt * 7 + pr) * 256 + c * 16 : OOB;
    const int vb = ((c & 1) ? L_SB : L_CB) + g * 16;
    {
      v4u bfv[N1C / 2];
#pragma unroll
      for (int s = 0; s < N1C / 2; ++s) bfv[s] = lds128(L, vb + s * 64);
      SB();
#pragma unroll
      for (int s = 0; s < N1C / 2; ++s) {
        u0 = mfma(w1f[2 * s], bfv[s], u0);
        u1 = mfma(w1f[2 * s + 1], bfv[s], u1);
      }
    }
    SB();
    bar();
    SB();
    issue<7>(xf, rws, xo);
    issue<1>(xs, rws, so);
    const int hro = w == 0 ? WS_FH + cl * 1024 + (lane & 1) * 4 : OOB;
    u32 hr = ld32c(rws, hro, 0);
    SB();
    settle<7>(xf, rws, xo, budget);
    settle<1>(xs, rws, so, budget);
    if (w == 0) {
      while ((__builtin_amdgcn_ballot_w64(hr != tag) != 0ull) & (budget > 0)) {
        asm volatile("" ::: "memory");
        __builtin_amdgcn_s_sleep(PSLEEP);
        hr = ld32c(rws, hro, 0);
        --budget;
      }
    }
#pragma unroll
    for (int n = N1C; n < 28; ++n) w1_load(n);
    SB();
#pragma unroll
    for (int i = 0; i < 7; ++i) sts128(L, L_A + (rt * 14 + 7 * kx + i) * 1024 + l16, xf[i]);
    const float s1 = gsum(asf(xs[0][0]) + asf(xs[0][2])), s2 = gsum(asf(xs[0][1]) + asf(xs[0][3]));
    if (g == 0) sts64(L, L_ST + ((rt * 2 + kx) * 16 + c) * 8, v2u{asu(s1), asu(s2)});
    SB();
#pragma unroll
    for (int n = 0; n < 4; ++n) w2_load(n);
  }
  bar();
  v4u hf[4][7];
  const int ho = rank < 7 ? h_cl + pw * 28672 + l16 : OOB;
  if (w < 7) {
    v4u xf[4];
#pragma unroll
    for (int i = 0; i < 4; ++i) xf[i] = lds128(L, L_A + i * 1024 + l16);
    const int ub = L_UV + w * 256 + g * 64;
    const int vb = ((c & 1) ? L_SB : L_CB) + g * 16;
    f32x4 uu[2], vv[2];
    SB();
    const float nl2e = -1.44269504f;
#pragma unroll
    for (int r = 0; r < 4; ++r) {
      f32x4 a[2] = {z4, z4};
      v2u sta, stb;
#pragma unroll
      for (int s = 0; s < 14; ++s) {
        const int n = r * 14 + s;
        a[0] = mfma(w1f[2 * s], xf[n & 3], a[0]);
        a[1] = mfma(w1f[2 * s + 1], xf[n & 3], a[1]);
        if (n + 4 < 56) xf[n & 3] = lds128(L, L_A + (n + 4) * 1024 + l16);
        if (s == 8) {
          sta = *(LAS v2u*)(L + L_ST + (r * 32 + c) * 8);
          stb = *(LAS v2u*)(L + L_ST + (r * 32 + 16 + c) * 8);
        }
        if (r == 0 && s >= N1C / 2) {
          const v4u bv = lds128(L, vb + s * 64);
          u0 = mfma(w1f[2 * s], bv, u0);
          u1 = mfma(w1f[2 * s + 1], bv, u1);
        }
        if (n >= 4 && n % 2 == 0 && 4 + (n - 4) / 2 < N2B) w2_load(4 + (n - 4) / 2);
        if (r == 3 && s >= 7) hf[0][s - 7] = ld128c(rws, ho + ((s - 7) & 3) * 1024, ((s - 7) >> 2) * 4096);
        SB();
      }
      if (r == 0) {
        const float sc = (c & 1) ? nl2e : 1.f;
        if (c < 2) {
          stsf4(L, ub + c * 32, u0 * sc);
          stsf4(L, ub + c * 32 + 16, u1 * sc);
        }
        uu[0] = ldsf4(L, ub);
        uu[1] = ldsf4(L, ub + 16);
        vv[0] = ldsf4(L, ub + 32);
        vv[1] = ldsf4(L, ub + 48);
      }
      const float mu = (asf(sta[0]) + asf(stb[0])) * (1.f / D);
      const float var = fmaxf((asf(sta[1]) + asf(stb[1])) * (1.f / D) - mu * mu, 0.f);
      const float rsl = nl2e * __builtin_amdgcn_rsqf(var + 1e-5f);
      float y[8];
#pragma unroll
      for (int e = 0; e < 8; ++e) {
        const float z = vfma(vfma(-mu, uu[e & 1][e >> 1], a[e & 1][e >> 1]), rsl, vv[e & 1][e >> 1]);
        y[e] = z * __builtin_amdgcn_rcpf(vfma(__builtin_amdgcn_exp2f(z), nl2e, nl2e));
      }
      const v4u hv = {canon(pkn(y[0], y[1])), canon(pkn(y[2], y[3])), canon(pkn(y[4], y[5])), canon(pkn(y[6], y[7]))};
      if (r == 3) {
        lds_wait(L, L_GO + 8, 1u, budget);
      }
      st128c(hv, rws, l16, h_cl + (rank * 28 + w + r * 7) * 1024);
      SB();
    }
  } else {
    if (rank == 7) {
      const v4u sv = {SENT, SENT, SENT, SENT};
#pragma unroll 1
      for (int p = 0; p < 8; ++p) {
#pragma unroll
        for (int i = 0; i < 7; ++i) st128c(sv, rws, l16 + i * 1024, h_cl + p * 28672 + 21504);
      }
      drain();
      st32c(tag, rws, lane == 0 ? WS_FH + cl * 1024 + 8 : OOB, 0);
      *(volatile LAS u32*)(L + L_GO + 8) = 1u;
    }
#pragma unroll
    for (int n = 4; n < N2B; ++n) w2_load(n);
    if (rank < 7) {
      bool seen = false;
#pragma unroll 1
      for (int ww = 0; ww < 8; ++ww) {
        u32 f;
        asm volatile("s_nop 4\n\tbuffer_load_dword %0, off, %1, %2 sc1" : "=v"(f) : "s"(rsw), "s"(WS_FH + cl * 1024 + 8) : "memory");
#pragma unroll
        for (int q = 0; q < NST; ++q) dma1k(rsw, l16, WS_P2 + rank * 229376 + (ww * 28 + N2B + q) * 1024, L_W2 + (ww * NST + q) * 1024);
        asm volatile("s_waitcnt vmcnt(%1)" : "+v"(f) : "n"(NST) : "memory");
        if (__builtin_amdgcn_readfirstlane(f) == tag) {
          *(volatile LAS u32*)(L + L_GO + 8) = 1u;
          seen = true;
        }
      }
      while (!seen & (budget > 0)) {
        asm volatile("" ::: "memory");
        seen = __builtin_amdgcn_readfirstlane(ld32c(rws, WS_FH + cl * 1024 + 8, 0)) == tag;
        --budget;
      }
      *(volatile LAS u32*)(L + L_GO + 8) = 1u;
    }
    drain_seen();
  }
  if (w == 7) issue<7>(hf[0], rws, ho);
  if (rank < 7) {
    bar();
    issue<7>(hf[1], rws, ho + 7168);
#pragma unroll
    for (int n = 0; n < NST; ++n) w2f[N2B + n] = lds128(L, L_W2 + (pw * NST + n) * 1024 + l16);
    SB();
#pragma unroll
    for (int r = 0; r < 4; ++r) {
      settle<7>(hf[r], rws, ho + r * 7168, budget);
      if (r < 2) issue<7>(hf[r + 2], rws, ho + (r + 2) * 7168);
      SB();
      f32x4 acc[4];
#pragma unroll
      for (int q = 0; q < 4; ++q) acc[q] = z4;
#pragma unroll
      for (int s = 0; s < 7; ++s) {
#pragma unroll
        for (int q = 0; q < 4; ++q) acc[q] = mfma(w2f[4 * s + q], hf[r][s], acc[q]);
      }
      SB();
      if (r == 1) bar();
#pragma unroll
      for (int q = 0; q < 4; ++q) stsf4(L, ((r * 8 + w) * 4 + q) * 1024 + l16, acc[q]);
      SB();
    }
    const v4u gb = lds128(L, L_GB + rank * 128 + kx * 64 + g * 16);
    f32x2 gbf[4], x1f[4];
#pragma unroll
    for (int i = 0; i < 4; ++i) {
      gbf[i] = up2(gb[i]);
      x1f[i] = up2(x1p[i]);
      asm volatile("" : "+v"(gbf[i]), "+v"(x1f[i]));
    }
    bar();
    f32x4 pa[8][2];
#pragma unroll
    for (int p = 0; p < 8; ++p) {
      pa[p][0] = ldsf4(L, ((rt * 8 + p) * 4 + 2 * kx) * 1024 + l16);
      pa[p][1] = ldsf4(L, ((rt * 8 + p) * 4 + 2 * kx + 1) * 1024 + l16);
    }
    SB();
    f32x4 a0 = pa[0][0], a1 = pa[0][1];
#pragma unroll
    for (int p = 1; p < 8; ++p) {
      a0 += pa[p][0];
      a1 += pa[p][1];
    }
    v4u ov;
#pragma unroll
    for (int i = 0; i < 4; ++i) {
      ov[i] = pkn(vfma(gbf[i].x, a0[i], x1f[i].x), vfma(gbf[i].y, a1[i], x1f[i].y));
    }
    const rsrc_t rout = mkrs(pout, S * D * 2);
    const auto e0 = __builtin_amdgcn_permlane16_swap(ov[0], ov[2], false, false), e1 = __builtin_amdgcn_permlane16_swap(ov[1], ov[3], false, false);
    const int oo = c * 896 + (g & 1) * 32 + (g >> 1) * 16 + (t0 + 16 * rt) * 896 + rank * 128 + kx * 64;
    __builtin_amdgcn_raw_buffer_store_b128(v4u{e0[0], e1[0], e0[1], e1[1]}, rout, oo, 0, 0);
  } else if (cl == 0 && w == 7) {
    for (;;) {
      asm volatile("" ::: "memory");
      bool ok = true;
#pragma unroll
      for (int i = 0; i < 4; ++i) {
        const v4u f = ld128c(rws, l16 + i * 1024, WS_FB);
        ok = ok & (f[0] == tag) & (f[1] == tag) & (f[2] == tag) & (f[3] == tag);
      }
      --budget;
      if ((__builtin_amdgcn_ballot_w64(!ok) == 0ull) | (budget <= 0)) break;
      __builtin_amdgcn_s_sleep(8);
    }
    if (lane == 0) st32c(tag, rws, WS_EPOCH, 0);
  }
  asm volatile(".Lfk_pc1:");
}
}

extern "C" void kernel_launch(void* const* d_in, const int* in_sizes, int n_in, void* d_out, int out_size, void* d_ws,
                              size_t ws_size, hipStream_t stream) {
  (void)hipFuncSetAttribute((const void*)fk::fused, hipFuncAttributeMaxDynamicSharedMemorySize, fk::L_END);
  fk::fused<<<256, 512, fk::L_END, stream>>>(d_in[0], d_in[1], d_in[2], d_in[4], d_in[5], d_in[6], d_in[7], d_out, d_ws);
}
```
